```python
import torch

def _softmax(x, axis=-1):
    return torch.softmax(x, dim=axis)
def _gelu(x):
    return torch.nn.functional.gelu(x, approximate="tanh")
def _silu(x):
    return torch.nn.functional.silu(x)
def _sigmoid(x):
    return torch.sigmoid(x)
def _layernorm(x, axis=-1, eps=1e-5):
    m = x.mean(dim=axis, keepdim=True)
    v = x.var(dim=axis, keepdim=True, unbiased=False)
    return (x - m) / torch.sqrt(v + eps)
def _scatter_add(tgt, idx, src, axis=0):
    return tgt.clone().index_add_(axis, idx, src)
def _segment_sum(src, seg, n):
    out = torch.zeros((n,) + src.shape[1:], dtype=src.dtype)
    return out.index_add_(0, seg, src)
def _pad(x, pw):
    flat = tuple(v for pair in reversed(pw) for v in pair)
    return torch.nn.functional.pad(x, flat)

kernel_name = 'parallel_sum__spectral_fnet__dense_activation_relu__prompt'

def setup_inputs():

    _w = (lambda a: (lambda t: t.to(torch.bfloat16) if t.is_floating_point() else t)(torch.as_tensor(a)))
    def _A_setup():
        B, S, D = 1, 2048, 576
        ns = torch.arange(S)[:, None] * torch.arange(S)[None, :]
        nd = torch.arange(D)[:, None] * torch.arange(D)[None, :]
        Ws_re = torch.cos(-2.0 * torch.pi * ns / S).to(float)
        Ws_im = torch.sin(-2.0 * torch.pi * ns / S).to(float)
        Wd_re = torch.cos(-2.0 * torch.pi * nd / D).to(float)
        Wd_im = torch.sin(-2.0 * torch.pi * nd / D).to(float)
        return {
            "x": _w((lambda *s: torch.randn(*s, dtype=torch.bfloat16) * 0.05)(B, S, D)),
            "dft_seq_re": _w(Ws_re),
            "dft_seq_im": _w(Ws_im),
            "dft_hid_re": _w(Wd_re),
            "dft_hid_im": _w(Wd_im),
        }
    _A_in = _A_setup()

    def _B_setup():
        B, S, D, Dff = 1, 2048, 576, 4608
        return {
            "x": _w((lambda *s: torch.randn(*s, dtype=torch.bfloat16) * 0.05)(B, S, D)),
            "w1": _w((lambda *s: torch.randn(*s, dtype=torch.bfloat16) * 0.4)(D, Dff)),
            "w2": _w((lambda *s: torch.randn(*s, dtype=torch.bfloat16) * 0.4)(Dff, D)),
        }
    _B_in = _B_setup()

    return {
        "A_x": _A_in["x"],
        "A_dft_seq_re": _A_in["dft_seq_re"],
        "A_dft_seq_im": _A_in["dft_seq_im"],
        "A_dft_hid_re": _A_in["dft_hid_re"],
        "A_dft_hid_im": _A_in["dft_hid_im"],
        "B_x": _B_in["x"],
        "B_w1": _B_in["w1"],
        "B_w2": _B_in["w2"],
    }

def reference(A_x, A_dft_seq_re, A_dft_seq_im, A_dft_hid_re, A_dft_hid_im, B_x, B_w1, B_w2):
    def _A_ref(x, dft_seq_re, dft_seq_im, dft_hid_re, dft_hid_im):
        a = torch.einsum("bsd,ed->bse", x, dft_hid_re)
        b = torch.einsum("bsd,ed->bse", x, dft_hid_im)
        y = torch.einsum("ts,bse->bte", dft_seq_re, a) - torch.einsum("ts,bse->bte", dft_seq_im, b)
        return y

    def _B_ref(x, w1, w2):
        h = torch.einsum("bsd,df->bsf", x, w1)
        zero = torch.zeros_like(h)
        h = torch.maximum(h, zero)
        return torch.einsum("bsf,fd->bsd", h, w2)

    x = A_x
    xn = _layernorm(x, axis=-1)
    return x + _A_ref(xn, A_dft_seq_re, A_dft_seq_im, A_dft_hid_re, A_dft_hid_im) + _B_ref(xn, B_w1, B_w2)
```

```cpp
#include <hip/hip_runtime.h>
#include <hip/hip_bf16.h>
typedef unsigned short u16;
typedef short bf16x8 __attribute__((ext_vector_type(8)));
typedef float f32x4 __attribute__((ext_vector_type(4)));
constexpr int S = 2048, D = 576, F = 4608;

__device__ __forceinline__ float bf2f(u16 v) { return __uint_as_float(((unsigned)v) << 16); }
__device__ __forceinline__ u16 f2bf(float f) { __hip_bfloat16 b = __float2bfloat16(f); return *reinterpret_cast<u16*>(&b); }

__global__ __launch_bounds__(256) void ln_kernel(const u16* __restrict__ x, u16* __restrict__ xn) {
    int row = blockIdx.x * 4 + (threadIdx.x >> 6), lane = threadIdx.x & 63;
    const u16* xr = x + (size_t)row * D;
    float v[9]; float s = 0.f;
#pragma unroll
    for (int i = 0; i < 9; ++i) { v[i] = bf2f(xr[lane + 64 * i]); s += v[i]; }
#pragma unroll
    for (int o = 32; o >= 1; o >>= 1) s += __shfl_xor(s, o);
    float m = s * (1.0f / D); float q = 0.f;
#pragma unroll
    for (int i = 0; i < 9; ++i) { float d = v[i] - m; q += d * d; }
#pragma unroll
    for (int o = 32; o >= 1; o >>= 1) q += __shfl_xor(q, o);
    float r = 1.0f / sqrtf(q * (1.0f / D) + 1e-5f);
#pragma unroll
    for (int i = 0; i < 9; ++i) xn[(size_t)row * D + lane + 64 * i] = f2bf((v[i] - m) * r);
}

template <bool BKN, int MODE>
__global__ __launch_bounds__(256) void gemm_kernel(const u16* __restrict__ A, int lda, const u16* __restrict__ B, int ldb,
                                                   void* __restrict__ C, int ldc, int K, int ksplit_len, size_t c_split_stride) {
    constexpr int BM = 128, BN = 64, BK = 32;
    __shared__ __attribute__((aligned(16))) u16 sA[BM][BK + 8];
    __shared__ __attribute__((aligned(16))) u16 sB[BN][BK + 8];
    int bm = blockIdx.y * BM, bn = blockIdx.x * BN;
    int k0 = blockIdx.z * ksplit_len, k1 = min(K, k0 + ksplit_len);
    int tid = threadIdx.x, wid = tid >> 6, lane = tid & 63, fr = lane & 15, fq = lane >> 4;
    f32x4 acc[2][4];
    for (int i = 0; i < 2; ++i) for (int j = 0; j < 4; ++j) acc[i][j] = (f32x4){0, 0, 0, 0};
    for (int k = k0; k < k1; k += BK) {
        for (int i = 0; i < 2; ++i) { int e = tid + i * 256, r = e >> 2, c = (e & 3) * 8;
            *(bf16x8*)&sA[r][c] = *(const bf16x8*)(A + (size_t)(bm + r) * lda + k + c); }
        if (BKN) { int r = tid >> 3, c = (tid & 7) * 8;
            bf16x8 v = *(const bf16x8*)(B + (size_t)(k + r) * ldb + bn + c);
            for (int j = 0; j < 8; ++j) sB[c + j][r] = (u16)v[j];
        } else { int r = tid >> 2, c = (tid & 3) * 8;
            *(bf16x8*)&sB[r][c] = *(const bf16x8*)(B + (size_t)(bn + r) * ldb + k + c); }
        __syncthreads();
        bf16x8 a[2], b[4];
        for (int i = 0; i < 2; ++i) a[i] = *(const bf16x8*)&sA[wid * 32 + i * 16 + fr][fq * 8];
        for (int j = 0; j < 4; ++j) b[j] = *(const bf16x8*)&sB[j * 16 + fr][fq * 8];
        for (int i = 0; i < 2; ++i) for (int j = 0; j < 4; ++j)
            acc[i][j] = __builtin_amdgcn_mfma_f32_16x16x32_bf16(a[i], b[j], acc[i][j], 0, 0, 0);
        __syncthreads();
    }
    for (int i = 0; i < 2; ++i) for (int j = 0; j < 4; ++j) for (int r = 0; r < 4; ++r) {
        int row = bm + wid * 32 + i * 16 + fq * 4 + r, col = bn + j * 16 + fr; float v = acc[i][j][r];
        if (MODE == 3) ((float*)C)[blockIdx.z * c_split_stride + (size_t)row * ldc + col] = v;
        else { if (MODE == 1) v = -v; if (MODE == 2) v = fmaxf(v, 0.f); ((u16*)C)[(size_t)row * ldc + col] = f2bf(v); }
    }
}

__global__ __launch_bounds__(256) void final_kernel(const u16* __restrict__ x, const float* __restrict__ p, int np, u16* __restrict__ out) {
    int i = blockIdx.x * 256 + threadIdx.x;
    float s = bf2f(x[i]);
    for (int j = 0; j < np; ++j) s += p[(size_t)j * S * D + i];
    out[i] = f2bf(s);
}

extern "C" void kernel_launch(void* const* d_in, const int* in_sizes, int n_in, void* d_out, int out_size, void* d_ws, size_t ws_size, hipStream_t stream) {
    const u16* x = (const u16*)d_in[0]; const u16* Cr = (const u16*)d_in[1]; const u16* Ci = (const u16*)d_in[2];
    const u16* Dr = (const u16*)d_in[3]; const u16* Di = (const u16*)d_in[4];
    const u16* w1 = (const u16*)d_in[6]; const u16* w2 = (const u16*)d_in[7];
    char* ws = (char*)d_ws;
    u16* xn = (u16*)ws; ws += (size_t)S * D * 2;
    u16* a = (u16*)ws; ws += (size_t)S * D * 2;
    u16* b = (u16*)ws; ws += (size_t)S * D * 2;
    u16* h = (u16*)ws; ws += (size_t)S * F * 2;
    float* p = (float*)ws;
    size_t ps = (size_t)S * D;
    ln_kernel<<<S / 4, 256, 0, stream>>>(x, xn);
    gemm_kernel<false, 0><<<dim3(D / 64, S / 128, 1), 256, 0, stream>>>(xn, D, Dr, D, a, D, D, D, 0);
    gemm_kernel<false, 1><<<dim3(D / 64, S / 128, 1), 256, 0, stream>>>(xn, D, Di, D, b, D, D, D, 0);
    gemm_kernel<true, 2><<<dim3(F / 64, S / 128, 1), 256, 0, stream>>>(xn, D, w1, F, h, F, D, D, 0);
    gemm_kernel<true, 3><<<dim3(D / 64, S / 128, 2), 256, 0, stream>>>(Cr, S, a, D, p, D, S, 1024, ps);
    gemm_kernel<true, 3><<<dim3(D / 64, S / 128, 2), 256, 0, stream>>>(Ci, S, b, D, p + 2 * ps, D, S, 1024, ps);
    gemm_kernel<true, 3><<<dim3(D / 64, S / 128, 4), 256, 0, stream>>>(h, F, w2, D, p + 4 * ps, D, F, 1152, ps);
    final_kernel<<<S * D / 256, 256, 0, stream>>>(x, p, 8, (u16*)d_out);
}
```

```cpp
#include <hip/hip_runtime.h>
#include <hip/hip_bf16.h>
typedef unsigned short u16;
typedef short bf16x8 __attribute__((ext_vector_type(8)));
typedef float f32x4 __attribute__((ext_vector_type(4)));
constexpr int S = 2048, D = 576, F = 4608;

__device__ __forceinline__ float bf2f(u16 v) { return __uint_as_float(((unsigned)v) << 16); }
__device__ __forceinline__ u16 f2bf(float f) { __hip_bfloat16 b = __float2bfloat16(f); return *reinterpret_cast<u16*>(&b); }

__global__ __launch_bounds__(256) void ln_kernel(const u16* __restrict__ x, u16* __restrict__ xn) {
    int row = blockIdx.x * 4 + (threadIdx.x >> 6), lane = threadIdx.x & 63;
    const u16* xr = x + (size_t)row * D;
    float v[9]; float s = 0.f;
#pragma unroll
    for (int i = 0; i < 9; ++i) { v[i] = bf2f(xr[lane + 64 * i]); s += v[i]; }
#pragma unroll
    for (int o = 32; o >= 1; o >>= 1) s += __shfl_xor(s, o);
    float m = s * (1.0f / D); float q = 0.f;
#pragma unroll
    for (int i = 0; i < 9; ++i) { float d = v[i] - m; q += d * d; }
#pragma unroll
    for (int o = 32; o >= 1; o >>= 1) q += __shfl_xor(q, o);
    float r = 1.0f / sqrtf(q * (1.0f / D) + 1e-5f);
#pragma unroll
    for (int i = 0; i < 9; ++i) xn[(size_t)row * D + lane + 64 * i] = f2bf((v[i] - m) * r);
}

template <bool BKN, int MODE>
__global__ __launch_bounds__(256) void gemm_kernel(const u16* __restrict__ A, int lda, const u16* __restrict__ B, int ldb,
                                                   void* __restrict__ C, int ldc, int K, int ksplit_len, size_t c_split_stride) {
    constexpr int BM = 128, BN = 64, BK = 32;
    __shared__ __attribute__((aligned(16))) u16 sA[BM][BK + 8];
    __shared__ __attribute__((aligned(16))) u16 sB[BN][BK + 8];
    int bm = blockIdx.y * BM, bn = blockIdx.x * BN;
    int k0 = blockIdx.z * ksplit_len, k1 = min(K, k0 + ksplit_len);
    int tid = threadIdx.x, wid = tid >> 6, lane = tid & 63, fr = lane & 15, fq = lane >> 4;
    f32x4 acc[2][4];
    for (int i = 0; i < 2; ++i) for (int j = 0; j < 4; ++j) acc[i][j] = (f32x4){0, 0, 0, 0};
    for (int k = k0; k < k1; k += BK) {
        for (int i = 0; i < 2; ++i) { int e = tid + i * 256, r = e >> 2, c = (e & 3) * 8;
            *(bf16x8*)&sA[r][c] = *(const bf16x8*)(A + (size_t)(bm + r) * lda + k + c); }
        if (BKN) { int r = tid >> 3, c = (tid & 7) * 8;
            bf16x8 v = *(const bf16x8*)(B + (size_t)(k + r) * ldb + bn + c);
            for (int j = 0; j < 8; ++j) sB[c + j][r] = (u16)v[j];
        } else { int r = tid >> 2, c = (tid & 3) * 8;
            *(bf16x8*)&sB[r][c] = *(const bf16x8*)(B + (size_t)(bn + r) * ldb + k + c); }
        __syncthreads();
        bf16x8 a[2], b[4];
        for (int i = 0; i < 2; ++i) a[i] = *(const bf16x8*)&sA[wid * 32 + i * 16 + fr][fq * 8];
        for (int j = 0; j < 4; ++j) b[j] = *(const bf16x8*)&sB[j * 16 + fr][fq * 8];
        for (int i = 0; i < 2; ++i) for (int j = 0; j < 4; ++j)
            acc[i][j] = __builtin_amdgcn_mfma_f32_16x16x32_bf16(a[i], b[j], acc[i][j], 0, 0, 0);
        __syncthreads();
    }
    for (int i = 0; i < 2; ++i) for (int j = 0; j < 4; ++j) for (int r = 0; r < 4; ++r) {
        int row = bm + wid * 32 + i * 16 + fq * 4 + r, col = bn + j * 16 + fr; float v = acc[i][j][r];
        if (MODE == 3) ((float*)C)[blockIdx.z * c_split_stride + (size_t)row * ldc + col] = v;
        else { if (MODE == 1) v = -v; if (MODE == 2) v = fmaxf(v, 0.f); ((u16*)C)[(size_t)row * ldc + col] = f2bf(v); }
    }
}

__global__ __launch_bounds__(256) void final_kernel(const u16* __restrict__ x, const float* __restrict__ p, int np, u16* __restrict__ out) {
    int i = blockIdx.x * 256 + threadIdx.x;
    float s = bf2f(x[i]);
    for (int j = 0; j < np; ++j) s += p[(size_t)j * S * D + i];
    out[i] = f2bf(s);
}


__global__ void probe_kernel(const char* __restrict__ p, int n, int stride, unsigned* sink) {
    if (threadIdx.x != 0) return;
    unsigned off = 0, acc = 0;
    for (int i = 0; i < n; ++i) {
        unsigned v = *(const volatile unsigned*)(p + off);
        unsigned z; asm volatile("v_and_b32 %0, 0, %1" : "=v"(z) : "v"(v));
        acc += v; off += stride + z;
    }
    sink[0] = acc;
}

extern "C" void kernel_launch(void* const* d_in, const int* in_sizes, int n_in, void* d_out, int out_size, void* d_ws, size_t ws_size, hipStream_t stream) {
    const u16* x = (const u16*)d_in[0]; const u16* Cr = (const u16*)d_in[1]; const u16* Ci = (const u16*)d_in[2];
    const u16* Dr = (const u16*)d_in[3]; const u16* Di = (const u16*)d_in[4];
    const u16* w1 = (const u16*)d_in[6]; const u16* w2 = (const u16*)d_in[7];
    char* ws = (char*)d_ws;
    u16* xn = (u16*)ws; ws += (size_t)S * D * 2;
    u16* a = (u16*)ws; ws += (size_t)S * D * 2;
    u16* b = (u16*)ws; ws += (size_t)S * D * 2;
    u16* h = (u16*)ws; ws += (size_t)S * F * 2;
    float* p = (float*)ws;
    size_t ps = (size_t)S * D;
    probe_kernel<<<1, 64, 0, stream>>>((const char*)d_ws + (70u << 20), 2048, 1152, (unsigned*)((char*)d_ws + ws_size - 64));
    ln_kernel<<<S / 4, 256, 0, stream>>>(x, xn);
    gemm_kernel<false, 0><<<dim3(D / 64, S / 128, 1), 256, 0, stream>>>(xn, D, Dr, D, a, D, D, D, 0);
    gemm_kernel<false, 1><<<dim3(D / 64, S / 128, 1), 256, 0, stream>>>(xn, D, Di, D, b, D, D, D, 0);
    gemm_kernel<true, 2><<<dim3(F / 64, S / 128, 1), 256, 0, stream>>>(xn, D, w1, F, h, F, D, D, 0);
    gemm_kernel<true, 3><<<dim3(D / 64, S / 128, 2), 256, 0, stream>>>(Cr, S, a, D, p, D, S, 1024, ps);
    gemm_kernel<true, 3><<<dim3(D / 64, S / 128, 2), 256, 0, stream>>>(Ci, S, b, D, p + 2 * ps, D, S, 1024, ps);
    gemm_kernel<true, 3><<<dim3(D / 64, S / 128, 4), 256, 0, stream>>>(h, F, w2, D, p + 4 * ps, D, F, 1152, ps);
    final_kernel<<<S * D / 256, 256, 0, stream>>>(x, p, 8, (u16*)d_out);
}
```

```cpp
#include <hip/hip_runtime.h>
typedef unsigned short u16;
typedef unsigned u32;
typedef short bf16x8 __attribute__((ext_vector_type(8)));
typedef short s16x4 __attribute__((ext_vector_type(4)));
typedef __bf16 bf16x2 __attribute__((ext_vector_type(2)));
typedef float f32x4 __attribute__((ext_vector_type(4)));
typedef float f32x2 __attribute__((ext_vector_type(2)));
typedef unsigned u32x4 __attribute__((ext_vector_type(4)));
typedef unsigned u32x2 __attribute__((ext_vector_type(2)));
#define LAS __attribute__((address_space(3)))
#define DEVI __device__ __forceinline__

constexpr int S_ = 2048, D_ = 576, F_ = 4608;
constexpr u32 WS_EPOCH = 0;
constexpr u32 WS_FAB = 256;
constexpr u32 WS_FW = 1024;
constexpr u32 WS_FH = 2048;
constexpr u32 WS_FP = 4096;
constexpr u32 WS_STAT = 8192;
constexpr u32 WS_BM = 1u << 20;
constexpr u32 WS_H = 12u << 20;
constexpr u32 WS_P = 32u << 20;
constexpr u32 WS_END = 42u << 20;
constexpr u32 L_B1 = 96 * 1024;
constexpr u32 L_B2 = 64 * 1024;
constexpr u32 L_STAT = 150 * 1024;
constexpr u32 L_CS = 152 * 1024;
constexpr u32 LDS_BYTES = 153 * 1024;
constexpr int SPIN_MAX = 1 << 20;

extern __shared__ __attribute__((aligned(16))) char smem[];

#define WAIT_V(n) asm volatile("s_waitcnt vmcnt(" #n ")" ::: "memory")
#define WAIT_L0() asm volatile("s_waitcnt lgkmcnt(0)" ::: "memory")
#define BAR() __builtin_amdgcn_s_barrier()

#define SB() __builtin_amdgcn_sched_barrier(0)
#define DSR128(dst, addr, off) asm volatile("ds_read_b128 %0, %1 offset:%2" : "=v"(dst) : "v"(addr), "i"(off) : "memory")
#define DSRTR(dst, addr, off) asm volatile("ds_read_b64_tr_b16 %0, %1 offset:%2" : "=v"(dst) : "v"(addr), "i"(off) : "memory")
#define CAT(lo, hi) __builtin_shufflevector(lo, hi, 0, 1, 2, 3, 4, 5, 6, 7)
typedef __amdgpu_buffer_rsrc_t rsrc_t;
DEVI rsrc_t mk_rsrc(const void* p, u32 bytes) { return __builtin_amdgcn_make_buffer_rsrc((void*)p, 0, bytes, 0x00020000); }
template <int AUX> DEVI void dma(rsrc_t rs, u32 lds, u32 voff, u32 soff) {
    __builtin_amdgcn_raw_ptr_buffer_load_lds(rs, (LAS void*)((LAS char*)smem + lds), 16, voff, soff, 0, AUX);
}
DEVI bf16x8 lds128(u32 a) { return *(LAS bf16x8*)((LAS char*)smem + a); }
DEVI bf16x8 ldstr(u32 a0, u32 a1) {
    s16x4 lo = __builtin_amdgcn_ds_read_tr16_b64_v4i16((LAS s16x4*)((LAS char*)smem + a0));
    s16x4 hi = __builtin_amdgcn_ds_read_tr16_b64_v4i16((LAS s16x4*)((LAS char*)smem + a1));
    return __builtin_shufflevector(lo, hi, 0, 1, 2, 3, 4, 5, 6, 7);
}
DEVI f32x4 mfma(bf16x8 a, bf16x8 b, f32x4 c) { return __builtin_amdgcn_mfma_f32_16x16x32_bf16(a, b, c, 0, 0, 0); }
DEVI u32 pk2(float a, float b) { bf16x2 v = {(__bf16)a, (__bf16)b}; return __builtin_bit_cast(u32, v); }
DEVI float bflo(u32 v) { return __uint_as_float(v << 16); }
DEVI float bfhi(u32 v) { return __uint_as_float(v & 0xffff0000u); }
DEVI float dot1(u32 v, float acc) {
    return __builtin_amdgcn_fdot2_f32_bf16(__builtin_bit_cast(bf16x2, v), __builtin_bit_cast(bf16x2, 0x3f803f80u), acc, false);
}
#define DOT1(acc, v) acc = dot1(v, acc)
#define DOT4(acc, v) do { DOT1(acc, v[0]); DOT1(acc, v[1]); DOT1(acc, v[2]); DOT1(acc, v[3]); } while (0)
template <int CTRL> DEVI float dpp_add(float v) {
    return v + __builtin_bit_cast(float, __builtin_amdgcn_update_dpp(0, __builtin_bit_cast(int, v), CTRL, 0xf, 0xf, true));
}
DEVI float wave_sum(float v) {
    v = dpp_add<0xB1>(v); v = dpp_add<0x4E>(v); v = dpp_add<0x141>(v); v = dpp_add<0x140>(v);
    int iv = __builtin_bit_cast(int, v);
    float a = __builtin_bit_cast(float, __builtin_amdgcn_readlane(iv, 0)), b = __builtin_bit_cast(float, __builtin_amdgcn_readlane(iv, 16));
    float c = __builtin_bit_cast(float, __builtin_amdgcn_readlane(iv, 32)), d = __builtin_bit_cast(float, __builtin_amdgcn_readlane(iv, 48));
    return (a + b) + (c + d);
}
DEVI u32 ld_flag(const char* p) { return __hip_atomic_load((const u32*)p, __ATOMIC_RELAXED, __HIP_MEMORY_SCOPE_AGENT); }
DEVI void st_flag(char* p, u32 v) { __hip_atomic_store((u32*)p, v, __ATOMIC_RELAXED, __HIP_MEMORY_SCOPE_AGENT); }
DEVI void poll(const char* p, bool act, u32 tag) {
    for (int it = 0; it < SPIN_MAX; ++it) {
        u32 f = tag;
        if (act) f = ld_flag(p);
        if (__all(f == tag)) break;
        __builtin_amdgcn_s_sleep(2);
    }
}

__global__ __launch_bounds__(512, 2) void fused_kernel(const u16* __restrict__ x, const u16* __restrict__ Cr, const u16* __restrict__ Ci,
                                                       const u16* __restrict__ Dr, const u16* __restrict__ Di, const u16* __restrict__ w1,
                                                       const u16* __restrict__ w2, u16* __restrict__ out, char* __restrict__ ws) {
    const int tid = threadIdx.x, l = tid & 63, w = __builtin_amdgcn_readfirstlane(tid >> 6);
    const int bid = blockIdx.x, xcd = bid & 7, g = bid >> 3, kap = xcd >> 1, mu = xcd & 1;
    const int q = l >> 4, r16 = l & 15;
    const bool is_ab = g < 8;
    const rsrc_t rs_ws = mk_rsrc(ws, WS_END);
    const rsrc_t rs_x = mk_rsrc(x, S_ * D_ * 2);
    const rsrc_t rs_c = mk_rsrc((kap >> 1) ? Ci : Cr, S_ * S_ * 2);

    u32 epoch; u32x4 rowv; u32 rowt;
    {
        const u16* xr = x + (size_t)(bid * 8 + w) * D_;
        asm volatile("global_load_dword %0, %1, off sc1" : "=v"(epoch) : "v"(ws + WS_EPOCH) : "memory");
        asm volatile("global_load_dwordx4 %0, %1, off" : "=v"(rowv) : "v"(xr + l * 8) : "memory");
        asm volatile("global_load_ushort %0, %1, off" : "=v"(rowt) : "v"(xr + 512 + l) : "memory");
    }

    const int mb = g >> 2, nb = g & 3;
    const int t2 = 1024 * mu + 128 * mb;
    const u32 vC0 = (u32)(16 * w + (l >> 3)) * 4096u + 16u * ((l & 7) ^ q);
    const u32 vC1 = (u32)(16 * w + 8 + (l >> 3)) * 4096u + 16u * ((l & 7) ^ (4 + q));
    const u32 sC = (u32)t2 * 4096u + 1024u * (kap & 1);

    const int wm = w & 3, wn = w >> 2;
    u32 tag;
    int row0;

    if (!is_ab) {
        const int gg = g - 8, rb = gg / 6, cb = gg - rb * 6;
        row0 = 1024 * mu + 256 * rb;
        const int f0 = 1152 * kap + 192 * cb;
        const rsrc_t rs_w = mk_rsrc(w1, D_ * F_ * 2);
        u32 vA0 = (u32)(16 * w + (l >> 3)) * 1152u + 16u * ((l & 7) ^ q);
        u32 vA1 = (u32)(16 * w + 8 + (l >> 3)) * 1152u + 16u * ((l & 7) ^ (4 + q));
        const u32 sA = (u32)row0 * 1152u;
        u32 vB0, vB1;
        {
            auto boff = [&](int p, int ll) -> u32 {
                int cg = p >> 2, g4 = p & 3, a8 = ll >> 3, c8 = ll & 7;
                int k = 16 * (g4 >> 1) + 8 * (a8 >> 2) + 4 * (g4 & 1) + (a8 & 3);
                int jj = (c8 >> 1) ^ ((a8 >> 1) & 3);
                return (u32)k * 9216u + (u32)(f0 * 2 + cg * 128 + 32 * jj + 16 * (c8 & 1));
            };
            vB0 = boff(w, l);
            vB1 = boff(8 + (w >> 1), (l & 31) + 32 * (w & 1));
        }
        const u32 lB0 = L_B1 + w * 1024, lB1 = L_B1 + (8 + (w >> 1)) * 1024 + (w & 1) * 512;
        auto issueA = [&](int unit, int hf) {
            u32 ld = (unit % 3) * 32768u + (16 * hf + 2 * w) * 1024u, so = sA + hf * (128u * 1152u) + unit * 128u;
            dma<0>(rs_x, ld, vA0, so); dma<0>(rs_x, ld + 1024, vA1, so);
        };
        auto issueB = [&](int u) {
            u32 so = (u32)u * (32u * 9216u), lo = (u & 3) * 12288u;
            dma<0>(rs_w, lB0 + lo, vB0, so);
            if (l < 32) dma<0>(rs_w, lB1 + lo, vB1, so);
        };
        issueA(0, 0); issueA(0, 1); issueB(0); issueB(1); issueA(1, 0); issueA(1, 1); issueB(2); issueB(3); issueA(2, 0); issueA(2, 1);

        asm volatile("s_waitcnt vmcnt(20)" : "+v"(epoch), "+v"(rowv), "+v"(rowt) :: "memory");
        tag = __builtin_amdgcn_readfirstlane(epoch) + 1u;
        {
            float v[9];
#pragma unroll
            for (int i = 0; i < 4; ++i) { v[2 * i] = bflo(rowv[i]); v[2 * i + 1] = bfhi(rowv[i]); }
            v[8] = bflo(rowt);
            float s = 0.f;
#pragma unroll
            for (int i = 0; i < 9; ++i) s += v[i];
            float mean = wave_sum(s) * (1.f / D_), ss = 0.f;
#pragma unroll
            for (int i = 0; i < 9; ++i) { float d = v[i] - mean; ss += d * d; }
            float rstd = 1.0f / sqrtf(wave_sum(ss) * (1.f / D_) + 1e-5f);
            u32x4 gr = {__float_as_uint(mean), tag, __float_as_uint(rstd), tag};
            if (l == 0) __builtin_amdgcn_raw_buffer_store_b128(gr, rs_ws, WS_STAT + (bid * 8 + w) * 16, 0, 16);
        }

        const u32 rA = (u32)(64 * wm + r16) * 128u + 16u * (q ^ (r16 >> 1));
        u32 rB[6];
        {
            int a8 = 4 * (q & 1) + ((l >> 2) & 3);
#pragma unroll
            for (int t = 0; t < 6; ++t) {
                int j = 6 * wn + t;
                rB[t] = L_B1 + (j >> 2) * 4096 + (q >> 1) * 2048 + a8 * 128 + 32 * ((j & 3) ^ ((a8 >> 1) & 3)) + 8 * (l & 3);
            }
        }
        f32x4 acc[4][6];
#pragma unroll
        for (int i = 0; i < 4; ++i)
#pragma unroll
            for (int t = 0; t < 6; ++t) acc[i][t] = (f32x4){0.f, 0.f, 0.f, 0.f};
        float csA = 0.f, csB = 0.f;
        bf16x8 fa[2][4]; s16x4 fl[2][6], fh[2][6];
        auto step = [&](const int set, int u) {
            if (u < 15) WAIT_V(8); else WAIT_V(4);
            WAIT_L0(); BAR(); SB();
            bf16x8 fb[6];
#pragma unroll
            for (int t = 0; t < 6; ++t) fb[t] = CAT(fl[set][t], fh[set][t]);
            const int un = (u < 17) ? u + 1 : u;
            const u32 ab = ((un >> 1) % 3) * 32768u + (rA ^ ((un & 1) * 64u)), bo = (un & 3) * 12288u;
            acc[0][0] = mfma(fb[0], fa[set][0], acc[0][0]); acc[1][0] = mfma(fb[0], fa[set][1], acc[1][0]); SB();
            if (u >= 1 && u <= 12) issueA(((u - 1) >> 1) + 3, set ^ 1);
            else if (u >= 13 && u <= 16) {
                int j = u - 13; u32 ld = j * 16384u + 2 * w * 1024u, so = sC + 128u * j;
                dma<0>(rs_c, ld, vC0, so); dma<0>(rs_c, ld + 1024, vC1, so);
            }
            SB();
            acc[2][0] = mfma(fb[0], fa[set][2], acc[2][0]); acc[3][0] = mfma(fb[0], fa[set][3], acc[3][0]); SB();
            if (u <= 13) issueB(u + 4);
            SB();
#pragma unroll
            for (int m = 4; m < 24; ++m) {
                const int t = m >> 2, i = m & 3, rr = m - 4;
                acc[i][t] = mfma(fb[t], fa[set][i], acc[i][t]);
                if (rr < 4) DSR128(fa[set ^ 1][rr], ab, 2048 * rr);
                else if (rr < 16) { const int tt = (rr - 4) >> 1; u32 ad = rB[tt] + bo; if (rr & 1) DSRTR(fh[set ^ 1][tt], ad, 1024); else DSRTR(fl[set ^ 1][tt], ad, 0); }
                SB();
            }
            const u32x4 b0 = __builtin_bit_cast(u32x4, fb[0]), b1 = __builtin_bit_cast(u32x4, fb[1]), b2 = __builtin_bit_cast(u32x4, fb[2]);
            const u32x4 b3 = __builtin_bit_cast(u32x4, fb[3]), b4 = __builtin_bit_cast(u32x4, fb[4]), b5 = __builtin_bit_cast(u32x4, fb[5]);
            if (wm == 0) { DOT4(csA, b0); DOT1(csB, b1[0]); DOT1(csB, b1[1]); }
            else if (wm == 1) { DOT4(csA, b2); DOT1(csB, b1[2]); DOT1(csB, b1[3]); }
            else if (wm == 2) { DOT4(csA, b3); DOT1(csB, b4[0]); DOT1(csB, b4[1]); }
            else { DOT4(csA, b5); DOT1(csB, b4[2]); DOT1(csB, b4[3]); }
        };
        WAIT_V(15); BAR();
#pragma unroll
        for (int i = 0; i < 4; ++i) DSR128(fa[0][i], rA, 2048 * i);
#pragma unroll
        for (int t = 0; t < 6; ++t) { DSRTR(fl[0][t], rB[t], 0); DSRTR(fh[0][t], rB[t], 1024); }
#pragma unroll 1
        for (int u = 0; u < 18; u += 2) { step(0, u); step(1, u + 1); }
        asm volatile("s_waitcnt lgkmcnt(0)" :: "v"(fa[0][0]), "v"(fa[0][1]), "v"(fa[0][2]), "v"(fa[0][3]), "v"(fl[0][0]), "v"(fl[0][1]), "v"(fl[0][2]), "v"(fl[0][3]),
                     "v"(fl[0][4]), "v"(fl[0][5]), "v"(fh[0][0]), "v"(fh[0][1]), "v"(fh[0][2]), "v"(fh[0][3]), "v"(fh[0][4]), "v"(fh[0][5]) : "memory");

        {
            float a = csA, b = csB;
            a += __shfl_xor(a, 16); a += __shfl_xor(a, 32); b += __shfl_xor(b, 16); b += __shfl_xor(b, 32);
            int ta = (wm == 0) ? 0 : (wm == 1) ? 2 : (wm == 2) ? 3 : 5, tb = (wm < 2) ? 1 : 4;
            if (l < 16) {
                *(LAS float*)((LAS char*)smem + L_CS + (96 * wn + 16 * ta + l) * 4) = a;
                *(LAS float*)((LAS char*)smem + L_CS + ((wm & 1) ? 192 + 32 * wn + 16 * (wm >> 1) + l : 96 * wn + 16 * tb + l) * 4) = b;
            }
        }
        if (w < 4) {
            u32x4 gr;
            for (int it = 0; it < SPIN_MAX; ++it) {
                gr = __builtin_amdgcn_raw_buffer_load_b128(rs_ws, WS_STAT + (row0 + tid) * 16, 0, 16);
                if (__all(gr[1] == tag && gr[3] == tag)) break;
                __builtin_amdgcn_s_sleep(2);
            }
            *(LAS u32x2*)((LAS char*)smem + L_STAT + tid * 8) = (u32x2){gr[0], gr[2]};
        }
        WAIT_L0(); BAR();
        {
            f32x4 cs[6];
#pragma unroll
            for (int t = 0; t < 6; ++t) cs[t] = *(LAS f32x4*)((LAS char*)smem + L_CS + (96 * wn + 16 * t + 4 * q) * 4);
            cs[1] += *(LAS f32x4*)((LAS char*)smem + L_CS + (192 + 32 * wn + 4 * q) * 4);
            cs[4] += *(LAS f32x4*)((LAS char*)smem + L_CS + (192 + 32 * wn + 16 + 4 * q) * 4);
            const u32 hb = WS_H + (u32)(((row0 + 64 * wm) >> 7) * 72) * 16384u + l * 16;
#pragma unroll
            for (int i = 0; i < 4; ++i) {
                f32x2 st = *(LAS f32x2*)((LAS char*)smem + L_STAT + (64 * wm + 16 * i + r16) * 8);
#pragma unroll
                for (int P = 0; P < 3; ++P) {
                    float v[8];
#pragma unroll
                    for (int e = 0; e < 4; ++e) {
                        v[e] = fmaxf((acc[i][2 * P][e] - st[0] * cs[2 * P][e]) * st[1], 0.f);
                        v[4 + e] = fmaxf((acc[i][2 * P + 1][e] - st[0] * cs[2 * P + 1][e]) * st[1], 0.f);
                    }
                    u32x4 o = {pk2(v[0], v[1]), pk2(v[2], v[3]), pk2(v[4], v[5]), pk2(v[6], v[7])};
                    int k32 = (f0 + 96 * wn) / 32 + P;
                    u32 so = (u32)(k32 >> 1) * 16384u + (u32)((4 * (wm & 1) + i) * 2 + (k32 & 1)) * 1024u;
                    __builtin_amdgcn_raw_buffer_store_b128(o, rs_ws, hb + so, 0, 16);
                }
            }
        }
        WAIT_V(0); BAR();
        if (tid == 0) st_flag(ws + WS_FH + ((xcd * 4 + rb) * 8 + cb) * 4, tag);
    } else {
        const int mat = kap >> 1;
        row0 = 1024 * mu + 512 * (kap & 1) + 256 * (g >> 2);
        const rsrc_t rs_d = mk_rsrc(mat ? Di : Dr, D_ * D_ * 2);
        const int rperm = 32 * (w >> 1) + 4 * (w & 1) + 8 * (l >> 5) + ((l >> 3) & 3);
        u32 vA0 = (u32)rperm * 1152u + 16u * ((l & 7) ^ q);
        u32 vA1 = (u32)(rperm + 16) * 1152u + 16u * ((l & 7) ^ (4 + q));
        const u32 sA = (u32)row0 * 1152u;
        u32 vB[2], vBs[2];
#pragma unroll
        for (int hf = 0; hf < 2; ++hf) {
            int p = 9 * hf + w, rho = 8 * p + (l >> 3);
            vB[hf] = (u32)(144 * nb + rho) * 1152u + 16u * ((l & 7) ^ ((rho >> 1) & 7));
            int rho2 = 8 * (9 * hf + 8) + w;
            vBs[hf] = (u32)(144 * nb + rho2) * 1152u + 16u * ((l & 7) ^ ((rho2 >> 1) & 7));
        }
        auto issueA = [&](int unit, int hf) {
            u32 ld = (unit % 3) * 32768u + (16 * hf + 2 * w) * 1024u, so = sA + hf * (128u * 1152u) + unit * 128u;
            dma<0>(rs_x, ld, vA0, so); dma<0>(rs_x, ld + 1024, vA1, so);
        };
        auto issueB = [&](int unit, int hf) {
            u32 lb = L_B1 + (unit % 3) * 18432u + 9216u * hf, so = unit * 128u;
            dma<0>(rs_d, lb + w * 1024, hf ? vB[1] : vB[0], so);
            if (l < 8) dma<0>(rs_d, lb + 8192 + w * 128, hf ? vBs[1] : vBs[0], so);
        };
#pragma unroll
        for (int un = 0; un < 3; ++un) { issueA(un, 0); issueA(un, 1); issueB(un, 0); issueB(un, 1); }

        asm volatile("s_waitcnt vmcnt(24)" : "+v"(epoch), "+v"(rowv), "+v"(rowt) :: "memory");
        tag = __builtin_amdgcn_readfirstlane(epoch) + 1u;
        {
            float v[9];
#pragma unroll
            for (int i = 0; i < 4; ++i) { v[2 * i] = bflo(rowv[i]); v[2 * i + 1] = bfhi(rowv[i]); }
            v[8] = bflo(rowt);
            float s = 0.f;
#pragma unroll
            for (int i = 0; i < 9; ++i) s += v[i];
            float mean = wave_sum(s) * (1.f / D_), ss = 0.f;
#pragma unroll
            for (int i = 0; i < 9; ++i) { float d = v[i] - mean; ss += d * d; }
            float rstd = 1.0f / sqrtf(wave_sum(ss) * (1.f / D_) + 1e-5f);
            u32x4 gr = {__float_as_uint(mean), tag, __float_as_uint(rstd), tag};
            if (l == 0) __builtin_amdgcn_raw_buffer_store_b128(gr, rs_ws, WS_STAT + (bid * 8 + w) * 16, 0, 16);
        }

        const u32 sw = 16u * (q ^ (r16 >> 1));
        const u32 rA01 = (u32)(64 * wm + 32 * wn + r16) * 128u + sw, rA23 = (u32)(64 * wm + 32 * (1 - wn) + r16) * 128u + sw;
        const u32 rB0 = L_B1 + (u32)(64 * wn + r16) * 128u + sw, rB8 = L_B1 + (u32)(128 + r16) * 128u + sw;
        f32x4 acc[4][4], acc8[2];
#pragma unroll
        for (int i = 0; i < 4; ++i)
#pragma unroll
            for (int t = 0; t < 4; ++t) acc[i][t] = (f32x4){0.f, 0.f, 0.f, 0.f};
        acc8[0] = acc8[1] = (f32x4){0.f, 0.f, 0.f, 0.f};
        float csA = 0.f, csB = 0.f;
        bf16x8 fa[2][4], fb[2][5];
        auto step = [&](const int set, int u) {
            if (u < 15) WAIT_V(8); else WAIT_V(4);
            WAIT_L0(); BAR(); SB();
            const int un = (u < 17) ? u + 1 : u;
            const u32 sl = (un >> 1) % 3, xs = (un & 1) * 64u;
            const u32 a01 = sl * 32768u + (rA01 ^ xs), a23 = sl * 32768u + (rA23 ^ xs), b0a = sl * 18432u + (rB0 ^ xs), b8a = sl * 18432u + (rB8 ^ xs);
            acc[0][0] = mfma(fa[set][0], fb[set][0], acc[0][0]); acc[1][0] = mfma(fa[set][1], fb[set][0], acc[1][0]); SB();
            if (u >= 1 && u <= 12) { issueA(((u - 1) >> 1) + 3, set ^ 1); issueB(((u - 1) >> 1) + 3, set ^ 1); }
            else if (u >= 13 && u <= 16) {
                int j = u - 13; u32 ld = j * 16384u + 2 * w * 1024u, so = sC + 128u * j;
                dma<0>(rs_c, ld, vC0, so); dma<0>(rs_c, ld + 1024, vC1, so);
            }
            SB();
#pragma unroll
            for (int m = 2; m < 18; ++m) {
                const int t = m >> 2, i = m & 3, rr = m - 2;
                if (t < 4) acc[i][t & 3] = mfma(fa[set][i], fb[set][t], acc[i][t & 3]); else acc8[i & 1] = mfma(fa[set][i], fb[set][4], acc8[i & 1]);
                if (rr < 2) DSR128(fa[set ^ 1][rr], a01, 2048 * rr);
                else if (rr < 4) DSR128(fa[set ^ 1][rr], a23, 2048 * (rr - 2));
                else if (rr < 8) DSR128(fb[set ^ 1][rr - 4], b0a, 2048 * (rr - 4));
                else if (rr == 8) DSR128(fb[set ^ 1][4], b8a, 0);
                SB();
            }
            const u32x4 b0 = __builtin_bit_cast(u32x4, fb[set][0]), b1 = __builtin_bit_cast(u32x4, fb[set][1]), b2 = __builtin_bit_cast(u32x4, fb[set][2]);
            const u32x4 b3 = __builtin_bit_cast(u32x4, fb[set][3]), b4 = __builtin_bit_cast(u32x4, fb[set][4]);
            if (wm == 0) { DOT4(csA, b0); DOT1(csB, b4[0]); }
            else if (wm == 1) { DOT4(csA, b1); DOT1(csB, b4[1]); }
            else if (wm == 2) { DOT4(csA, b2); DOT1(csB, b4[2]); }
            else { DOT4(csA, b3); DOT1(csB, b4[3]); }
        };
        WAIT_V(17); BAR();
        DSR128(fa[0][0], rA01, 0); DSR128(fa[0][1], rA01, 2048); DSR128(fa[0][2], rA23, 0); DSR128(fa[0][3], rA23, 2048);
#pragma unroll
        for (int t = 0; t < 4; ++t) DSR128(fb[0][t], rB0, 2048 * t);
        DSR128(fb[0][4], rB8, 0);
#pragma unroll 1
        for (int u = 0; u < 18; u += 2) { step(0, u); step(1, u + 1); }
        asm volatile("s_waitcnt lgkmcnt(0)" :: "v"(fa[0][0]), "v"(fa[0][1]), "v"(fa[0][2]), "v"(fa[0][3]), "v"(fb[0][0]), "v"(fb[0][1]), "v"(fb[0][2]), "v"(fb[0][3]), "v"(fb[0][4]) : "memory");
        BAR();

        const int qd = 2 * mu + (g >> 2);
        {
            const rsrc_t rs_w2 = mk_rsrc(w2, F_ * D_ * 2);
            const u32 so = (u32)(1152 * kap + 288 * qd) * 1152u + 288u * nb;
#pragma unroll 1
            for (int p = w; p < 81; p += 8) {
                u32 o = p * 1024 + l * 16, row = o / 288u, ch = o - row * 288u;
                dma<0>(rs_w2, L_B2 + p * 1024, row * 1152u + ch, so);
            }
        }
        {
            float a = csA, b = csB;
            a += __shfl_xor(a, 16); a += __shfl_xor(a, 32); b += __shfl_xor(b, 16); b += __shfl_xor(b, 32);
            if (l < 16) {
                *(LAS float*)((LAS char*)smem + L_CS + (64 * wn + 16 * wm + l) * 4) = a;
                if (wn == 0) *(LAS float*)((LAS char*)smem + L_CS + (128 + 16 * wm + l) * 4) = b;
            }
        }
        if (w < 4) {
            u32x4 gr;
            for (int it = 0; it < SPIN_MAX; ++it) {
                gr = __builtin_amdgcn_raw_buffer_load_b128(rs_ws, WS_STAT + (row0 + tid) * 16, 0, 16);
                if (__all(gr[1] == tag && gr[3] == tag)) break;
                __builtin_amdgcn_s_sleep(2);
            }
            *(LAS u32x2*)((LAS char*)smem + L_STAT + tid * 8) = (u32x2){gr[0], gr[2]};
        }
        WAIT_L0(); BAR();
        const u32 bmb = WS_BM + (u32)((kap * 4 + nb) * 68) * 9216u + l * 16;
        {
            float cs[5];
#pragma unroll
            for (int t = 0; t < 4; ++t) cs[t] = *(LAS float*)((LAS char*)smem + L_CS + (64 * wn + 16 * t + r16) * 4);
            {
                LAS float* c8 = (LAS float*)((LAS char*)smem + L_CS + (128 + r16) * 4);
                cs[4] = (c8[0] + c8[16]) + (c8[32] + c8[48]);
            }
            const float sgn = mat ? -1.f : 1.f;
#pragma unroll
            for (int Pi = 0; Pi < 2; ++Pi) {
                int sb = 64 * wm + 32 * ((Pi + wn) & 1);
                float mean[8], rs[8];
#pragma unroll
                for (int e = 0; e < 4; ++e) {
                    f32x4 st = *(LAS f32x4*)((LAS char*)smem + L_STAT + (sb + 8 * q + 2 * e) * 8);
                    mean[2 * e] = st[0]; rs[2 * e] = st[1] * sgn; mean[2 * e + 1] = st[2]; rs[2 * e + 1] = st[3] * sgn;
                }
                int kb = 16 * mu + 8 * (g >> 2) + (sb >> 5);
#pragma unroll
                for (int t = 0; t < 5; ++t) {
                    if (t == 4 && Pi == 1) break;
                    f32x4 lo = (t < 4) ? acc[2 * Pi][t & 3] : acc8[0], hi = (t < 4) ? acc[2 * Pi + 1][t & 3] : acc8[1];
                    float v[8];
#pragma unroll
                    for (int e = 0; e < 4; ++e) { v[e] = (lo[e] - mean[e] * cs[t]) * rs[e]; v[4 + e] = (hi[e] - mean[4 + e] * cs[t]) * rs[4 + e]; }
                    u32x4 o = {pk2(v[0], v[1]), pk2(v[2], v[3]), pk2(v[4], v[5]), pk2(v[6], v[7])};
                    int nf = (t < 4) ? 4 * wn + t : 8;
                    __builtin_amdgcn_raw_buffer_store_b128(o, rs_ws, bmb + (u32)(kb * 9 + nf) * 1024u, 0, 16);
                }
            }
        }
        WAIT_V(0); BAR();
        if (tid == 0) st_flag(ws + WS_FAB + ((kap * 4 + nb) * 4 + qd) * 4, tag);
        {
            const u32 ra = L_B2 + (u32)(4 * q + ((l >> 2) & 3)) * 288u + 8u * (l & 3);
#pragma unroll 1
            for (int b = w; b < 81; b += 8) {
                int kb = b / 9, nf = b - kb * 9;
                u32 a0 = ra + kb * 9216 + nf * 32;
                bf16x8 v = ldstr(a0, a0 + 4608);
                __builtin_amdgcn_raw_buffer_store_b128(__builtin_bit_cast(u32x4, v), rs_ws, bmb + (u32)((32 + 9 * qd) * 9 + b) * 1024u, 0, 16);
            }
        }
        WAIT_V(0); WAIT_L0(); BAR();
        if (tid == 0) st_flag(ws + WS_FW + ((kap * 4 + nb) * 4 + qd) * 4, tag);
    }

    if (bid == 255 && w == 7) {
#pragma unroll 1
        for (int c = 0; c < 4; ++c) poll(ws + WS_STAT + (size_t)(c * 64 + l) * 128 + 4, true, tag);
        if (l == 0) st_flag(ws + WS_EPOCH, tag);
    }

    {
        const int gam = w >> 2, mh = (w >> 1) & 1, nh = w & 1;
        const u32 sB = WS_BM + (u32)((kap * 4 + nb) * 68) * 9216u;
        const u32 vB = (u32)(2 * w) * 1024u + l * 16, vBq = (u32)(16 + (w >> 2)) * 1024u + (w & 3) * 256u + l * 16;
        auto issueB = [&](int j) {
            u32 lb = L_B2 + (j & 3) * 18432u, so = sB + (u32)j * 18432u;
            dma<16>(rs_ws, lb + 2 * w * 1024, vB, so); dma<16>(rs_ws, lb + 2 * w * 1024 + 1024, vB + 1024, so);
            if (l < 16) dma<16>(rs_ws, lb + (16 + (w >> 2)) * 1024 + (w & 3) * 256, vBq, so);
        };
        const u32 sH = WS_H + (u32)((t2 >> 7) * 72 + 18 * kap) * 16384u;
        auto issueA = [&](int j) {
            u32 ld = (j & 3) * 16384u + 2 * w * 1024u;
            if (j < 16) { u32 so = sC + 2048u * (j >> 3) + 128u * (j & 7); dma<0>(rs_c, ld, vC0, so); dma<0>(rs_c, ld + 1024, vC1, so); }
            else { u32 so = sH + (u32)(j - 16) * 16384u; dma<16>(rs_ws, ld, vB, so); dma<16>(rs_ws, ld + 1024, vB + 1024, so); }
        };
        poll(ws + WS_FAB + ((kap * 4 + nb) * 4 + (l & 3)) * 4, l < 4, tag);
        issueB(0); issueB(1); issueB(2); issueB(3);

        const u32 sw = 16u * ((4 * gam + q) ^ (r16 >> 1));
        u32 rA01 = (u32)(64 * mh + 32 * nh + r16) * 128u + sw, rA23 = (u32)(64 * mh + 32 * (1 - nh) + r16) * 128u + sw;
        const u32 rB0 = L_B2 + (u32)(gam * 9 + 4 * nh) * 1024u + l * 16, rB8 = L_B2 + (u32)(gam * 9 + 8) * 1024u + l * 16;
        f32x4 acc[4][4], acc8[2];
#pragma unroll
        for (int i = 0; i < 4; ++i)
#pragma unroll
            for (int t = 0; t < 4; ++t) acc[i][t] = (f32x4){0.f, 0.f, 0.f, 0.f};
        acc8[0] = acc8[1] = (f32x4){0.f, 0.f, 0.f, 0.f};
        bf16x8 fa[2][4], fb[2][5];
        auto step = [&](const int set, int i) {
            if (i <= 30) WAIT_V(10); else WAIT_V(0);
            WAIT_L0(); BAR(); SB();
            if (i == 15) {
                rA01 = (u32)((4 * mh + 2 * nh) * 2 + gam) * 1024u + l * 16; rA23 = (u32)((4 * mh + 2 * (1 - nh)) * 2 + gam) * 1024u + l * 16;
            }
            const u32 sl = ((i < 33) ? i + 1 : i) & 3;
            const u32 a01 = sl * 16384u + rA01, a23 = sl * 16384u + rA23, b0a = sl * 18432u + rB0, b8a = sl * 18432u + rB8;
            acc[0][0] = mfma(fb[set][0], fa[set][0], acc[0][0]); acc[1][0] = mfma(fb[set][0], fa[set][1], acc[1][0]); SB();
            if (i == 12) {
                const char* fp = (l < 8) ? ws + WS_FH + ((xcd * 4 + (mb >> 1)) * 8 + l) * 4 : ws + WS_FW + ((kap * 4 + nb) * 4 + (l & 3)) * 4;
                poll(fp, l < 6 || (l >= 8 && l < 12), tag);
            }
            if (i < 30) issueA(i + 4);
            SB();
            acc[2][0] = mfma(fb[set][0], fa[set][2], acc[2][0]); acc[3][0] = mfma(fb[set][0], fa[set][3], acc[3][0]); SB();
            if (i < 30) issueB(i + 4);
            SB();
#pragma unroll
            for (int m = 4; m < 18; ++m) {
                const int t = m >> 2, ii = m & 3, rr = m - 4;
                if (t < 4) acc[ii][t & 3] = mfma(fb[set][t], fa[set][ii], acc[ii][t & 3]); else acc8[ii & 1] = mfma(fb[set][4], fa[set][ii], acc8[ii & 1]);
                if (rr < 2) DSR128(fa[set ^ 1][rr], a01, 2048 * rr);
                else if (rr < 4) DSR128(fa[set ^ 1][rr], a23, 2048 * (rr - 2));
                else if (rr < 8) DSR128(fb[set ^ 1][rr - 4], b0a, 1024 * (rr - 4));
                else if (rr == 8) DSR128(fb[set ^ 1][4], b8a, 0);
                SB();
            }
        };
        WAIT_V(9); BAR();
        DSR128(fa[0][0], rA01, 0); DSR128(fa[0][1], rA01, 2048); DSR128(fa[0][2], rA23, 0); DSR128(fa[0][3], rA23, 2048);
#pragma unroll
        for (int t = 0; t < 4; ++t) DSR128(fb[0][t], rB0, 1024 * t);
        DSR128(fb[0][4], rB8, 0);
#pragma unroll 1
        for (int i = 0; i < 34; i += 2) { step(0, i); step(1, i + 1); }
        asm volatile("s_waitcnt lgkmcnt(0)" :: "v"(fa[0][0]), "v"(fa[0][1]), "v"(fa[0][2]), "v"(fa[0][3]), "v"(fb[0][0]), "v"(fb[0][1]), "v"(fb[0][2]), "v"(fb[0][3]), "v"(fb[0][4]) : "memory");

        const int wp = w & 3;
        const int tile = (mu * 8 + mb) * 4 + nb;
        auto blk = [&](int b) -> u32x4 {
            f32x4 lo = (b < 8) ? acc[2 * (b >> 2)][b & 3] : acc8[0], hi = (b < 8) ? acc[2 * (b >> 2) + 1][b & 3] : acc8[1];
            return (u32x4){pk2(lo[0], lo[1]), pk2(lo[2], lo[3]), pk2(hi[0], hi[1]), pk2(hi[2], hi[3])};
        };
        BAR();
        LAS u32x4* xb = (LAS u32x4*)((LAS char*)smem + (u32)(w * 9) * 1024u + l * 16);
        const bool own = (wp == kap);
#pragma unroll
        for (int b = 0; b < 9; ++b) {
            bool mine = (gam == 0) ? (b < 5) : (b >= 5);
            if (own || !mine) xb[b * 64] = blk(b);
        }
        WAIT_L0(); BAR();
        if (!own) {
            LAS u32x4* pb = (LAS u32x4*)((LAS char*)smem + (u32)((w ^ 4) * 9) * 1024u + l * 16);
            const u32 pso = WS_P + (u32)((tile * 36 + wp * 9) * 4 + kap) * 1024u;
#pragma unroll
            for (int b = 0; b < 9; ++b) {
                bool mine = (gam == 0) ? (b < 5) : (b >= 5);
                if (mine) {
                    u32x4 o = pb[b * 64];
                    f32x4 lo = (b < 8) ? acc[2 * (b >> 2)][b & 3] : acc8[0], hi = (b < 8) ? acc[2 * (b >> 2) + 1][b & 3] : acc8[1];
                    u32x4 s = {pk2(lo[0] + bflo(o[0]), lo[1] + bfhi(o[0])), pk2(lo[2] + bflo(o[1]), lo[3] + bfhi(o[1])),
                               pk2(hi[0] + bflo(o[2]), hi[1] + bfhi(o[2])), pk2(hi[2] + bflo(o[3]), hi[3] + bfhi(o[3]))};
                    __builtin_amdgcn_raw_buffer_store_b128(s, rs_ws, l * 16 + pso + b * 4096u, 0, 16);
                }
            }
        }
        WAIT_V(0); BAR();
        if (tid == 0) st_flag(ws + WS_FP + (tile * 4 + kap) * 4, tag);
        poll(ws + WS_FP + (tile * 4 + (l & 3)) * 4, l < 4, tag);
        const int omh = kap >> 1, onh = kap & 1;
#pragma unroll 1
        for (int b = w; b < 9; b += 8) {
            u32x4 p[3];
#pragma unroll
            for (int k = 0; k < 3; ++k) {
                int kk = (kap + 1 + k) & 3;
                p[k] = __builtin_amdgcn_raw_buffer_load_b128(rs_ws, l * 16, WS_P + (u32)((tile * 36 + kap * 9 + b) * 4 + kk) * 1024u, 16);
            }
            int P = (b < 8) ? (b >> 2) : 0, nf = (b < 8) ? 4 * onh + (b & 3) : 8;
            int trow = t2 + 64 * omh + 32 * ((P + onh) & 1) + r16, col = 144 * nb + 16 * nf + 4 * q;
            u32x2 x0 = *(const u32x2*)(x + (size_t)trow * D_ + col), x1 = *(const u32x2*)(x + (size_t)(trow + 16) * D_ + col);
            u32x4 o0 = *(LAS u32x4*)((LAS char*)smem + (u32)(kap * 9 + b) * 1024u + l * 16);
            u32x4 o1 = *(LAS u32x4*)((LAS char*)smem + (u32)((kap + 4) * 9 + b) * 1024u + l * 16);
            u32x4 xx = {x0[0], x0[1], x1[0], x1[1]};
            u32 rr[4];
#pragma unroll
            for (int d = 0; d < 4; ++d) {
                float lo = ((bflo(o0[d]) + bflo(o1[d])) + (bflo(p[0][d]) + bflo(p[1][d]))) + (bflo(p[2][d]) + bflo(xx[d]));
                float hi = ((bfhi(o0[d]) + bfhi(o1[d])) + (bfhi(p[0][d]) + bfhi(p[1][d]))) + (bfhi(p[2][d]) + bfhi(xx[d]));
                rr[d] = pk2(lo, hi);
            }
            *(u32x2*)(out + (size_t)trow * D_ + col) = (u32x2){rr[0], rr[1]};
            *(u32x2*)(out + (size_t)(trow + 16) * D_ + col) = (u32x2){rr[2], rr[3]};
        }
    }
}

extern "C" void kernel_launch(void* const* d_in, const int* in_sizes, int n_in, void* d_out, int out_size, void* d_ws, size_t ws_size,
                              hipStream_t stream) {
    (void)hipFuncSetAttribute((const void*)fused_kernel, hipFuncAttributeMaxDynamicSharedMemorySize, LDS_BYTES);
    hipLaunchKernelGGL(fused_kernel, dim3(256), dim3(512), LDS_BYTES, stream, (const u16*)d_in[0], (const u16*)d_in[1], (const u16*)d_in[2],
                       (const u16*)d_in[3], (const u16*)d_in[4], (const u16*)d_in[6], (const u16*)d_in[7], (u16*)d_out, (char*)d_ws);
}
```

```cpp
#define PA 0
#define PB 1
#define PW 100
#include <hip/hip_runtime.h>
typedef unsigned short u16;
typedef unsigned u32;
typedef short bf16x8 __attribute__((ext_vector_type(8)));
typedef short s16x4 __attribute__((ext_vector_type(4)));
typedef __bf16 bf16x2 __attribute__((ext_vector_type(2)));
typedef float f32x4 __attribute__((ext_vector_type(4)));
typedef float f32x2 __attribute__((ext_vector_type(2)));
typedef unsigned u32x4 __attribute__((ext_vector_type(4)));
typedef unsigned u32x2 __attribute__((ext_vector_type(2)));
#define LAS __attribute__((address_space(3)))
#define DEVI __device__ __forceinline__

constexpr int S_ = 2048, D_ = 576, F_ = 4608;
constexpr u32 WS_EPOCH = 0;
constexpr u32 WS_FAB = 256;
constexpr u32 WS_FW = 1024;
constexpr u32 WS_FH = 2048;
constexpr u32 WS_FP = 4096;
constexpr u32 WS_STAT = 8192;
constexpr u32 WS_BM = 1u << 20;
constexpr u32 WS_H = 12u << 20;
constexpr u32 WS_P = 32u << 20;
constexpr u32 WS_END = 42u << 20;
constexpr u32 L_B1 = 96 * 1024;
constexpr u32 L_B2 = 64 * 1024;
constexpr u32 L_STAT = 150 * 1024;
constexpr u32 L_CS = 152 * 1024;
constexpr u32 LDS_BYTES = 153 * 1024;
constexpr int SPIN_MAX = 1 << 20;

extern __shared__ __attribute__((aligned(16))) char smem[];

#define WAIT_V(n) asm volatile("s_waitcnt vmcnt(" #n ")" ::: "memory")
#define WAIT_L0() asm volatile("s_waitcnt lgkmcnt(0)" ::: "memory")
#define BAR() __builtin_amdgcn_s_barrier()
#define STAMP(i) do { if ((i) == PA) ta_ = __builtin_amdgcn_s_memrealtime(); if ((i) == PB) tb_ = __builtin_amdgcn_s_memrealtime(); } while (0)

#define SB() __builtin_amdgcn_sched_barrier(0)
#define DSR128(dst, addr, off) asm volatile("ds_read_b128 %0, %1 offset:%2" : "=v"(dst) : "v"(addr), "i"(off) : "memory")
#define DSRTR(dst, addr, off) asm volatile("ds_read_b64_tr_b16 %0, %1 offset:%2" : "=v"(dst) : "v"(addr), "i"(off) : "memory")
#define CAT(lo, hi) __builtin_shufflevector(lo, hi, 0, 1, 2, 3, 4, 5, 6, 7)
typedef __amdgpu_buffer_rsrc_t rsrc_t;
DEVI rsrc_t mk_rsrc(const void* p, u32 bytes) { return __builtin_amdgcn_make_buffer_rsrc((void*)p, 0, bytes, 0x00020000); }
template <int AUX> DEVI void dma(rsrc_t rs, u32 lds, u32 voff, u32 soff) {
    __builtin_amdgcn_raw_ptr_buffer_load_lds(rs, (LAS void*)((LAS char*)smem + lds), 16, voff, soff, 0, AUX);
}
DEVI bf16x8 lds128(u32 a) { return *(LAS bf16x8*)((LAS char*)smem + a); }
DEVI bf16x8 ldstr(u32 a0, u32 a1) {
    s16x4 lo = __builtin_amdgcn_ds_read_tr16_b64_v4i16((LAS s16x4*)((LAS char*)smem + a0));
    s16x4 hi = __builtin_amdgcn_ds_read_tr16_b64_v4i16((LAS s16x4*)((LAS char*)smem + a1));
    return __builtin_shufflevector(lo, hi, 0, 1, 2, 3, 4, 5, 6, 7);
}
DEVI f32x4 mfma(bf16x8 a, bf16x8 b, f32x4 c) { return __builtin_amdgcn_mfma_f32_16x16x32_bf16(a, b, c, 0, 0, 0); }
DEVI u32 pk2(float a, float b) { bf16x2 v = {(__bf16)a, (__bf16)b}; return __builtin_bit_cast(u32, v); }
DEVI float bflo(u32 v) { return __uint_as_float(v << 16); }
DEVI float bfhi(u32 v) { return __uint_as_float(v & 0xffff0000u); }
DEVI float dot1(u32 v, float acc) {
    return __builtin_amdgcn_fdot2_f32_bf16(__builtin_bit_cast(bf16x2, v), __builtin_bit_cast(bf16x2, 0x3f803f80u), acc, false);
}
#define DOT1(acc, v) acc = dot1(v, acc)
#define DOT4(acc, v) do { DOT1(acc, v[0]); DOT1(acc, v[1]); DOT1(acc, v[2]); DOT1(acc, v[3]); } while (0)
template <int CTRL> DEVI float dpp_add(float v) {
    return v + __builtin_bit_cast(float, __builtin_amdgcn_update_dpp(0, __builtin_bit_cast(int, v), CTRL, 0xf, 0xf, true));
}
DEVI float wave_sum(float v) {
    v = dpp_add<0xB1>(v); v = dpp_add<0x4E>(v); v = dpp_add<0x141>(v); v = dpp_add<0x140>(v);
    int iv = __builtin_bit_cast(int, v);
    float a = __builtin_bit_cast(float, __builtin_amdgcn_readlane(iv, 0)), b = __builtin_bit_cast(float, __builtin_amdgcn_readlane(iv, 16));
    float c = __builtin_bit_cast(float, __builtin_amdgcn_readlane(iv, 32)), d = __builtin_bit_cast(float, __builtin_amdgcn_readlane(iv, 48));
    return (a + b) + (c + d);
}
DEVI u32 ld_flag(const char* p) { return __hip_atomic_load((const u32*)p, __ATOMIC_RELAXED, __HIP_MEMORY_SCOPE_AGENT); }
DEVI void st_flag(char* p, u32 v) { __hip_atomic_store((u32*)p, v, __ATOMIC_RELAXED, __HIP_MEMORY_SCOPE_AGENT); }
DEVI void poll(const char* p, bool act, u32 tag) {
    for (int it = 0; it < SPIN_MAX; ++it) {
        u32 f = tag;
        if (act) f = ld_flag(p);
        if (__all(f == tag)) break;
        __builtin_amdgcn_s_sleep(2);
    }
}

__global__ __launch_bounds__(512, 2) void fused_kernel(const u16* __restrict__ x, const u16* __restrict__ Cr, const u16* __restrict__ Ci,
                                                       const u16* __restrict__ Dr, const u16* __restrict__ Di, const u16* __restrict__ w1,
                                                       const u16* __restrict__ w2, u16* __restrict__ out, char* __restrict__ ws) {
    const int tid = threadIdx.x, l = tid & 63, w = __builtin_amdgcn_readfirstlane(tid >> 6);
    const int bid = blockIdx.x, xcd = bid & 7, g = bid >> 3, kap = xcd >> 1, mu = xcd & 1;
    const int q = l >> 4, r16 = l & 15;
    const bool is_ab = g < 8;
    const rsrc_t rs_ws = mk_rsrc(ws, WS_END);
    const rsrc_t rs_x = mk_rsrc(x, S_ * D_ * 2);
    const rsrc_t rs_c = mk_rsrc((kap >> 1) ? Ci : Cr, S_ * S_ * 2);

    unsigned long long ta_ = 0, tb_ = 0;
    STAMP(0);
    u32 epoch; u32x4 rowv; u32 rowt;
    {
        const u16* xr = x + (size_t)(bid * 8 + w) * D_;
        asm volatile("global_load_dword %0, %1, off sc1" : "=v"(epoch) : "v"(ws + WS_EPOCH) : "memory");
        asm volatile("global_load_dwordx4 %0, %1, off" : "=v"(rowv) : "v"(xr + l * 8) : "memory");
        asm volatile("global_load_ushort %0, %1, off" : "=v"(rowt) : "v"(xr + 512 + l) : "memory");
    }

    const int mb = g >> 2, nb = g & 3;
    const int t2 = 1024 * mu + 128 * mb;
    const u32 vC0 = (u32)(16 * w + (l >> 3)) * 4096u + 16u * ((l & 7) ^ q);
    const u32 vC1 = (u32)(16 * w + 8 + (l >> 3)) * 4096u + 16u * ((l & 7) ^ (4 + q));
    const u32 sC = (u32)t2 * 4096u + 1024u * (kap & 1);

    const int wm = w & 3, wn = w >> 2;
    u32 tag;
    int row0;

    if (!is_ab) {
        const int gg = g - 8, rb = gg / 6, cb = gg - rb * 6;
        row0 = 1024 * mu + 256 * rb;
        const int f0 = 1152 * kap + 192 * cb;
        const rsrc_t rs_w = mk_rsrc(w1, D_ * F_ * 2);
        u32 vA0 = (u32)(16 * w + (l >> 3)) * 1152u + 16u * ((l & 7) ^ q);
        u32 vA1 = (u32)(16 * w + 8 + (l >> 3)) * 1152u + 16u * ((l & 7) ^ (4 + q));
        const u32 sA = (u32)row0 * 1152u;
        u32 vB0, vB1;
        {
            auto boff = [&](int p, int ll) -> u32 {
                int cg = p >> 2, g4 = p & 3, a8 = ll >> 3, c8 = ll & 7;
                int k = 16 * (g4 >> 1) + 8 * (a8 >> 2) + 4 * (g4 & 1) + (a8 & 3);
                int jj = (c8 >> 1) ^ ((a8 >> 1) & 3);
                return (u32)k * 9216u + (u32)(f0 * 2 + cg * 128 + 32 * jj + 16 * (c8 & 1));
            };
            vB0 = boff(w, l);
            vB1 = boff(8 + (w >> 1), (l & 31) + 32 * (w & 1));
        }
        const u32 lB0 = L_B1 + w * 1024, lB1 = L_B1 + (8 + (w >> 1)) * 1024 + (w & 1) * 512;
        auto issueA = [&](int unit, int hf) {
            u32 ld = (unit % 3) * 32768u + (16 * hf + 2 * w) * 1024u, so = sA + hf * (128u * 1152u) + unit * 128u;
            dma<0>(rs_x, ld, vA0, so); dma<0>(rs_x, ld + 1024, vA1, so);
        };
        auto issueB = [&](int u) {
            u32 so = (u32)u * (32u * 9216u), lo = (u & 3) * 12288u;
            dma<0>(rs_w, lB0 + lo, vB0, so);
            if (l < 32) dma<0>(rs_w, lB1 + lo, vB1, so);
        };
        issueA(0, 0); issueA(0, 1); issueB(0); issueB(1); issueA(1, 0); issueA(1, 1); issueB(2); issueB(3); issueA(2, 0); issueA(2, 1);

        asm volatile("s_waitcnt vmcnt(20)" : "+v"(epoch), "+v"(rowv), "+v"(rowt) :: "memory");
        tag = __builtin_amdgcn_readfirstlane(epoch) + 1u;
        {
            float v[9];
#pragma unroll
            for (int i = 0; i < 4; ++i) { v[2 * i] = bflo(rowv[i]); v[2 * i + 1] = bfhi(rowv[i]); }
            v[8] = bflo(rowt);
            float s = 0.f;
#pragma unroll
            for (int i = 0; i < 9; ++i) s += v[i];
            float mean = wave_sum(s) * (1.f / D_), ss = 0.f;
#pragma unroll
            for (int i = 0; i < 9; ++i) { float d = v[i] - mean; ss += d * d; }
            float rstd = 1.0f / sqrtf(wave_sum(ss) * (1.f / D_) + 1e-5f);
            u32x4 gr = {__float_as_uint(mean), tag, __float_as_uint(rstd), tag};
            if (l == 0) __builtin_amdgcn_raw_buffer_store_b128(gr, rs_ws, WS_STAT + (bid * 8 + w) * 16, 0, 16);
        }

        const u32 rA = (u32)(64 * wm + r16) * 128u + 16u * (q ^ (r16 >> 1));
        u32 rB[6];
        {
            int a8 = 4 * (q & 1) + ((l >> 2) & 3);
#pragma unroll
            for (int t = 0; t < 6; ++t) {
                int j = 6 * wn + t;
                rB[t] = L_B1 + (j >> 2) * 4096 + (q >> 1) * 2048 + a8 * 128 + 32 * ((j & 3) ^ ((a8 >> 1) & 3)) + 8 * (l & 3);
            }
        }
        f32x4 acc[4][6];
#pragma unroll
        for (int i = 0; i < 4; ++i)
#pragma unroll
            for (int t = 0; t < 6; ++t) acc[i][t] = (f32x4){0.f, 0.f, 0.f, 0.f};
        float csA = 0.f, csB = 0.f;
        bf16x8 fa[2][4]; s16x4 fl[2][6], fh[2][6];
        auto step = [&](const int set, int u) {
            if (u < 15) WAIT_V(8); else WAIT_V(4);
            WAIT_L0(); BAR(); SB();
            bf16x8 fb[6];
#pragma unroll
            for (int t = 0; t < 6; ++t) fb[t] = CAT(fl[set][t], fh[set][t]);
            const int un = (u < 17) ? u + 1 : u;
            const u32 ab = ((un >> 1) % 3) * 32768u + (rA ^ ((un & 1) * 64u)), bo = (un & 3) * 12288u;
            acc[0][0] = mfma(fb[0], fa[set][0], acc[0][0]); acc[1][0] = mfma(fb[0], fa[set][1], acc[1][0]); SB();
            if (u >= 1 && u <= 12) issueA(((u - 1) >> 1) + 3, set ^ 1);
            else if (u >= 13 && u <= 16) {
                int j = u - 13; u32 ld = j * 16384u + 2 * w * 1024u, so = sC + 128u * j;
                dma<0>(rs_c, ld, vC0, so); dma<0>(rs_c, ld + 1024, vC1, so);
            }
            SB();
            acc[2][0] = mfma(fb[0], fa[set][2], acc[2][0]); acc[3][0] = mfma(fb[0], fa[set][3], acc[3][0]); SB();
            if (u <= 13) issueB(u + 4);
            SB();
#pragma unroll
            for (int m = 4; m < 24; ++m) {
                const int t = m >> 2, i = m & 3, rr = m - 4;
                acc[i][t] = mfma(fb[t], fa[set][i], acc[i][t]);
                if (rr < 4) DSR128(fa[set ^ 1][rr], ab, 2048 * rr);
                else if (rr < 16) { const int tt = (rr - 4) >> 1; u32 ad = rB[tt] + bo; if (rr & 1) DSRTR(fh[set ^ 1][tt], ad, 1024); else DSRTR(fl[set ^ 1][tt], ad, 0); }
                SB();
            }
            const u32x4 b0 = __builtin_bit_cast(u32x4, fb[0]), b1 = __builtin_bit_cast(u32x4, fb[1]), b2 = __builtin_bit_cast(u32x4, fb[2]);
            const u32x4 b3 = __builtin_bit_cast(u32x4, fb[3]), b4 = __builtin_bit_cast(u32x4, fb[4]), b5 = __builtin_bit_cast(u32x4, fb[5]);
            if (wm == 0) { DOT4(csA, b0); DOT1(csB, b1[0]); DOT1(csB, b1[1]); }
            else if (wm == 1) { DOT4(csA, b2); DOT1(csB, b1[2]); DOT1(csB, b1[3]); }
            else if (wm == 2) { DOT4(csA, b3); DOT1(csB, b4[0]); DOT1(csB, b4[1]); }
            else { DOT4(csA, b5); DOT1(csB, b4[2]); DOT1(csB, b4[3]); }
        };
        WAIT_V(15); BAR();
        STAMP(1);
#pragma unroll
        for (int i = 0; i < 4; ++i) DSR128(fa[0][i], rA, 2048 * i);
#pragma unroll
        for (int t = 0; t < 6; ++t) { DSRTR(fl[0][t], rB[t], 0); DSRTR(fh[0][t], rB[t], 1024); }
#pragma unroll 1
        for (int u = 0; u < 18; u += 2) { step(0, u); step(1, u + 1); }
        asm volatile("s_waitcnt lgkmcnt(0)" :: "v"(fa[0][0]), "v"(fa[0][1]), "v"(fa[0][2]), "v"(fa[0][3]), "v"(fl[0][0]), "v"(fl[0][1]), "v"(fl[0][2]), "v"(fl[0][3]),
                     "v"(fl[0][4]), "v"(fl[0][5]), "v"(fh[0][0]), "v"(fh[0][1]), "v"(fh[0][2]), "v"(fh[0][3]), "v"(fh[0][4]), "v"(fh[0][5]) : "memory");

        STAMP(2);
        {
            float a = csA, b = csB;
            a += __shfl_xor(a, 16); a += __shfl_xor(a, 32); b += __shfl_xor(b, 16); b += __shfl_xor(b, 32);
            int ta = (wm == 0) ? 0 : (wm == 1) ? 2 : (wm == 2) ? 3 : 5, tb = (wm < 2) ? 1 : 4;
            if (l < 16) {
                *(LAS float*)((LAS char*)smem + L_CS + (96 * wn + 16 * ta + l) * 4) = a;
                *(LAS float*)((LAS char*)smem + L_CS + ((wm & 1) ? 192 + 32 * wn + 16 * (wm >> 1) + l : 96 * wn + 16 * tb + l) * 4) = b;
            }
        }
        if (w < 4) {
            u32x4 gr;
            for (int it = 0; it < SPIN_MAX; ++it) {
                gr = __builtin_amdgcn_raw_buffer_load_b128(rs_ws, WS_STAT + (row0 + tid) * 16, 0, 16);
                if (__all(gr[1] == tag && gr[3] == tag)) break;
                __builtin_amdgcn_s_sleep(2);
            }
            *(LAS u32x2*)((LAS char*)smem + L_STAT + tid * 8) = (u32x2){gr[0], gr[2]};
        }
        WAIT_L0(); BAR();
        STAMP(3);
        {
            f32x4 cs[6];
#pragma unroll
            for (int t = 0; t < 6; ++t) cs[t] = *(LAS f32x4*)((LAS char*)smem + L_CS + (96 * wn + 16 * t + 4 * q) * 4);
            cs[1] += *(LAS f32x4*)((LAS char*)smem + L_CS + (192 + 32 * wn + 4 * q) * 4);
            cs[4] += *(LAS f32x4*)((LAS char*)smem + L_CS + (192 + 32 * wn + 16 + 4 * q) * 4);
            const u32 hb = WS_H + (u32)(((row0 + 64 * wm) >> 7) * 72) * 16384u + l * 16;
#pragma unroll
            for (int i = 0; i < 4; ++i) {
                f32x2 st = *(LAS f32x2*)((LAS char*)smem + L_STAT + (64 * wm + 16 * i + r16) * 8);
#pragma unroll
                for (int P = 0; P < 3; ++P) {
                    float v[8];
#pragma unroll
                    for (int e = 0; e < 4; ++e) {
                        v[e] = fmaxf((acc[i][2 * P][e] - st[0] * cs[2 * P][e]) * st[1], 0.f);
                        v[4 + e] = fmaxf((acc[i][2 * P + 1][e] - st[0] * cs[2 * P + 1][e]) * st[1], 0.f);
                    }
                    u32x4 o = {pk2(v[0], v[1]), pk2(v[2], v[3]), pk2(v[4], v[5]), pk2(v[6], v[7])};
                    int k32 = (f0 + 96 * wn) / 32 + P;
                    u32 so = (u32)(k32 >> 1) * 16384u + (u32)((4 * (wm & 1) + i) * 2 + (k32 & 1)) * 1024u;
                    __builtin_amdgcn_raw_buffer_store_b128(o, rs_ws, hb + so, 0, 16);
                }
            }
        }
        WAIT_V(0); BAR();
        if (tid == 0) st_flag(ws + WS_FH + ((xcd * 4 + rb) * 8 + cb) * 4, tag);
        STAMP(4); STAMP(5);
    } else {
        const int mat = kap >> 1;
        row0 = 1024 * mu + 512 * (kap & 1) + 256 * (g >> 2);
        const rsrc_t rs_d = mk_rsrc(mat ? Di : Dr, D_ * D_ * 2);
        const int rperm = 32 * (w >> 1) + 4 * (w & 1) + 8 * (l >> 5) + ((l >> 3) & 3);
        u32 vA0 = (u32)rperm * 1152u + 16u * ((l & 7) ^ q);
        u32 vA1 = (u32)(rperm + 16) * 1152u + 16u * ((l & 7) ^ (4 + q));
        const u32 sA = (u32)row0 * 1152u;
        u32 vB[2], vBs[2];
#pragma unroll
        for (int hf = 0; hf < 2; ++hf) {
            int p = 9 * hf + w, rho = 8 * p + (l >> 3);
            vB[hf] = (u32)(144 * nb + rho) * 1152u + 16u * ((l & 7) ^ ((rho >> 1) & 7));
            int rho2 = 8 * (9 * hf + 8) + w;
            vBs[hf] = (u32)(144 * nb + rho2) * 1152u + 16u * ((l & 7) ^ ((rho2 >> 1) & 7));
        }
        auto issueA = [&](int unit, int hf) {
            u32 ld = (unit % 3) * 32768u + (16 * hf + 2 * w) * 1024u, so = sA + hf * (128u * 1152u) + unit * 128u;
            dma<0>(rs_x, ld, vA0, so); dma<0>(rs_x, ld + 1024, vA1, so);
        };
        auto issueB = [&](int unit, int hf) {
            u32 lb = L_B1 + (unit % 3) * 18432u + 9216u * hf, so = unit * 128u;
            dma<0>(rs_d, lb + w * 1024, hf ? vB[1] : vB[0], so);
            if (l < 8) dma<0>(rs_d, lb + 8192 + w * 128, hf ? vBs[1] : vBs[0], so);
        };
#pragma unroll
        for (int un = 0; un < 3; ++un) { issueA(un, 0); issueA(un, 1); issueB(un, 0); issueB(un, 1); }

        asm volatile("s_waitcnt vmcnt(24)" : "+v"(epoch), "+v"(rowv), "+v"(rowt) :: "memory");
        tag = __builtin_amdgcn_readfirstlane(epoch) + 1u;
        {
            float v[9];
#pragma unroll
            for (int i = 0; i < 4; ++i) { v[2 * i] = bflo(rowv[i]); v[2 * i + 1] = bfhi(rowv[i]); }
            v[8] = bflo(rowt);
            float s = 0.f;
#pragma unroll
            for (int i = 0; i < 9; ++i) s += v[i];
            float mean = wave_sum(s) * (1.f / D_), ss = 0.f;
#pragma unroll
            for (int i = 0; i < 9; ++i) { float d = v[i] - mean; ss += d * d; }
            float rstd = 1.0f / sqrtf(wave_sum(ss) * (1.f / D_) + 1e-5f);
            u32x4 gr = {__float_as_uint(mean), tag, __float_as_uint(rstd), tag};
            if (l == 0) __builtin_amdgcn_raw_buffer_store_b128(gr, rs_ws, WS_STAT + (bid * 8 + w) * 16, 0, 16);
        }

        const u32 sw = 16u * (q ^ (r16 >> 1));
        const u32 rA01 = (u32)(64 * wm + 32 * wn + r16) * 128u + sw, rA23 = (u32)(64 * wm + 32 * (1 - wn) + r16) * 128u + sw;
        const u32 rB0 = L_B1 + (u32)(64 * wn + r16) * 128u + sw, rB8 = L_B1 + (u32)(128 + r16) * 128u + sw;
        f32x4 acc[4][4], acc8[2];
#pragma unroll
        for (int i = 0; i < 4; ++i)
#pragma unroll
            for (int t = 0; t < 4; ++t) acc[i][t] = (f32x4){0.f, 0.f, 0.f, 0.f};
        acc8[0] = acc8[1] = (f32x4){0.f, 0.f, 0.f, 0.f};
        float csA = 0.f, csB = 0.f;
        bf16x8 fa[2][4], fb[2][5];
        auto step = [&](const int set, int u) {
            if (u < 15) WAIT_V(8); else WAIT_V(4);
            WAIT_L0(); BAR(); SB();
            const int un = (u < 17) ? u + 1 : u;
            const u32 sl = (un >> 1) % 3, xs = (un & 1) * 64u;
            const u32 a01 = sl * 32768u + (rA01 ^ xs), a23 = sl * 32768u + (rA23 ^ xs), b0a = sl * 18432u + (rB0 ^ xs), b8a = sl * 18432u + (rB8 ^ xs);
            acc[0][0] = mfma(fa[set][0], fb[set][0], acc[0][0]); acc[1][0] = mfma(fa[set][1], fb[set][0], acc[1][0]); SB();
            if (u >= 1 && u <= 12) { issueA(((u - 1) >> 1) + 3, set ^ 1); issueB(((u - 1) >> 1) + 3, set ^ 1); }
            else if (u >= 13 && u <= 16) {
                int j = u - 13; u32 ld = j * 16384u + 2 * w * 1024u, so = sC + 128u * j;
                dma<0>(rs_c, ld, vC0, so); dma<0>(rs_c, ld + 1024, vC1, so);
            }
            SB();
#pragma unroll
            for (int m = 2; m < 18; ++m) {
                const int t = m >> 2, i = m & 3, rr = m - 2;
                if (t < 4) acc[i][t & 3] = mfma(fa[set][i], fb[set][t], acc[i][t & 3]); else acc8[i & 1] = mfma(fa[set][i], fb[set][4], acc8[i & 1]);
                if (rr < 2) DSR128(fa[set ^ 1][rr], a01, 2048 * rr);
                else if (rr < 4) DSR128(fa[set ^ 1][rr], a23, 2048 * (rr - 2));
                else if (rr < 8) DSR128(fb[set ^ 1][rr - 4], b0a, 2048 * (rr - 4));
                else if (rr == 8) DSR128(fb[set ^ 1][4], b8a, 0);
                SB();
            }
            const u32x4 b0 = __builtin_bit_cast(u32x4, fb[set][0]), b1 = __builtin_bit_cast(u32x4, fb[set][1]), b2 = __builtin_bit_cast(u32x4, fb[set][2]);
            const u32x4 b3 = __builtin_bit_cast(u32x4, fb[set][3]), b4 = __builtin_bit_cast(u32x4, fb[set][4]);
            if (wm == 0) { DOT4(csA, b0); DOT1(csB, b4[0]); }
            else if (wm == 1) { DOT4(csA, b1); DOT1(csB, b4[1]); }
            else if (wm == 2) { DOT4(csA, b2); DOT1(csB, b4[2]); }
            else { DOT4(csA, b3); DOT1(csB, b4[3]); }
        };
        WAIT_V(17); BAR();
        STAMP(1);
        DSR128(fa[0][0], rA01, 0); DSR128(fa[0][1], rA01, 2048); DSR128(fa[0][2], rA23, 0); DSR128(fa[0][3], rA23, 2048);
#pragma unroll
        for (int t = 0; t < 4; ++t) DSR128(fb[0][t], rB0, 2048 * t);
        DSR128(fb[0][4], rB8, 0);
#pragma unroll 1
        for (int u = 0; u < 18; u += 2) { step(0, u); step(1, u + 1); }
        asm volatile("s_waitcnt lgkmcnt(0)" :: "v"(fa[0][0]), "v"(fa[0][1]), "v"(fa[0][2]), "v"(fa[0][3]), "v"(fb[0][0]), "v"(fb[0][1]), "v"(fb[0][2]), "v"(fb[0][3]), "v"(fb[0][4]) : "memory");
        BAR();

        STAMP(2);
        const int qd = 2 * mu + (g >> 2);
        {
            const rsrc_t rs_w2 = mk_rsrc(w2, F_ * D_ * 2);
            const u32 so = (u32)(1152 * kap + 288 * qd) * 1152u + 288u * nb;
#pragma unroll 1
            for (int p = w; p < 81; p += 8) {
                u32 o = p * 1024 + l * 16, row = o / 288u, ch = o - row * 288u;
                dma<0>(rs_w2, L_B2 + p * 1024, row * 1152u + ch, so);
            }
        }
        {
            float a = csA, b = csB;
            a += __shfl_xor(a, 16); a += __shfl_xor(a, 32); b += __shfl_xor(b, 16); b += __shfl_xor(b, 32);
            if (l < 16) {
                *(LAS float*)((LAS char*)smem + L_CS + (64 * wn + 16 * wm + l) * 4) = a;
                if (wn == 0) *(LAS float*)((LAS char*)smem + L_CS + (128 + 16 * wm + l) * 4) = b;
            }
        }
        if (w < 4) {
            u32x4 gr;
            for (int it = 0; it < SPIN_MAX; ++it) {
                gr = __builtin_amdgcn_raw_buffer_load_b128(rs_ws, WS_STAT + (row0 + tid) * 16, 0, 16);
                if (__all(gr[1] == tag && gr[3] == tag)) break;
                __builtin_amdgcn_s_sleep(2);
            }
            *(LAS u32x2*)((LAS char*)smem + L_STAT + tid * 8) = (u32x2){gr[0], gr[2]};
        }
        WAIT_L0(); BAR();
        STAMP(3);
        const u32 bmb = WS_BM + (u32)((kap * 4 + nb) * 68) * 9216u + l * 16;
        {
            float cs[5];
#pragma unroll
            for (int t = 0; t < 4; ++t) cs[t] = *(LAS float*)((LAS char*)smem + L_CS + (64 * wn + 16 * t + r16) * 4);
            {
                LAS float* c8 = (LAS float*)((LAS char*)smem + L_CS + (128 + r16) * 4);
                cs[4] = (c8[0] + c8[16]) + (c8[32] + c8[48]);
            }
            const float sgn = mat ? -1.f : 1.f;
#pragma unroll
            for (int Pi = 0; Pi < 2; ++Pi) {
                int sb = 64 * wm + 32 * ((Pi + wn) & 1);
                float mean[8], rs[8];
#pragma unroll
                for (int e = 0; e < 4; ++e) {
                    f32x4 st = *(LAS f32x4*)((LAS char*)smem + L_STAT + (sb + 8 * q + 2 * e) * 8);
                    mean[2 * e] = st[0]; rs[2 * e] = st[1] * sgn; mean[2 * e + 1] = st[2]; rs[2 * e + 1] = st[3] * sgn;
                }
                int kb = 16 * mu + 8 * (g >> 2) + (sb >> 5);
#pragma unroll
                for (int t = 0; t < 5; ++t) {
                    if (t == 4 && Pi == 1) break;
                    f32x4 lo = (t < 4) ? acc[2 * Pi][t & 3] : acc8[0], hi = (t < 4) ? acc[2 * Pi + 1][t & 3] : acc8[1];
                    float v[8];
#pragma unroll
                    for (int e = 0; e < 4; ++e) { v[e] = (lo[e] - mean[e] * cs[t]) * rs[e]; v[4 + e] = (hi[e] - mean[4 + e] * cs[t]) * rs[4 + e]; }
                    u32x4 o = {pk2(v[0], v[1]), pk2(v[2], v[3]), pk2(v[4], v[5]), pk2(v[6], v[7])};
                    int nf = (t < 4) ? 4 * wn + t : 8;
                    __builtin_amdgcn_raw_buffer_store_b128(o, rs_ws, bmb + (u32)(kb * 9 + nf) * 1024u, 0, 16);
                }
            }
        }
        WAIT_V(0); BAR();
        if (tid == 0) st_flag(ws + WS_FAB + ((kap * 4 + nb) * 4 + qd) * 4, tag);
        STAMP(4);
        {
            const u32 ra = L_B2 + (u32)(4 * q + ((l >> 2) & 3)) * 288u + 8u * (l & 3);
#pragma unroll 1
            for (int b = w; b < 81; b += 8) {
                int kb = b / 9, nf = b - kb * 9;
                u32 a0 = ra + kb * 9216 + nf * 32;
                bf16x8 v = ldstr(a0, a0 + 4608);
                __builtin_amdgcn_raw_buffer_store_b128(__builtin_bit_cast(u32x4, v), rs_ws, bmb + (u32)((32 + 9 * qd) * 9 + b) * 1024u, 0, 16);
            }
        }
        WAIT_V(0); WAIT_L0(); BAR();
        if (tid == 0) st_flag(ws + WS_FW + ((kap * 4 + nb) * 4 + qd) * 4, tag);
        STAMP(5);
    }

    if (bid == 255 && w == 7) {
#pragma unroll 1
        for (int c = 0; c < 4; ++c) poll(ws + WS_STAT + (size_t)(c * 64 + l) * 128 + 4, true, tag);
        if (l == 0) st_flag(ws + WS_EPOCH, tag);
    }

    {
        const int gam = w >> 2, mh = (w >> 1) & 1, nh = w & 1;
        const u32 sB = WS_BM + (u32)((kap * 4 + nb) * 68) * 9216u;
        const u32 vB = (u32)(2 * w) * 1024u + l * 16, vBq = (u32)(16 + (w >> 2)) * 1024u + (w & 3) * 256u + l * 16;
        auto issueB = [&](int j) {
            u32 lb = L_B2 + (j & 3) * 18432u, so = sB + (u32)j * 18432u;
            dma<16>(rs_ws, lb + 2 * w * 1024, vB, so); dma<16>(rs_ws, lb + 2 * w * 1024 + 1024, vB + 1024, so);
            if (l < 16) dma<16>(rs_ws, lb + (16 + (w >> 2)) * 1024 + (w & 3) * 256, vBq, so);
        };
        const u32 sH = WS_H + (u32)((t2 >> 7) * 72 + 18 * kap) * 16384u;
        auto issueA = [&](int j) {
            u32 ld = (j & 3) * 16384u + 2 * w * 1024u;
            if (j < 16) { u32 so = sC + 2048u * (j >> 3) + 128u * (j & 7); dma<0>(rs_c, ld, vC0, so); dma<0>(rs_c, ld + 1024, vC1, so); }
            else { u32 so = sH + (u32)(j - 16) * 16384u; dma<16>(rs_ws, ld, vB, so); dma<16>(rs_ws, ld + 1024, vB + 1024, so); }
        };
        poll(ws + WS_FAB + ((kap * 4 + nb) * 4 + (l & 3)) * 4, l < 4, tag);
        STAMP(6);
        issueB(0); issueB(1); issueB(2); issueB(3);

        const u32 sw = 16u * ((4 * gam + q) ^ (r16 >> 1));
        u32 rA01 = (u32)(64 * mh + 32 * nh + r16) * 128u + sw, rA23 = (u32)(64 * mh + 32 * (1 - nh) + r16) * 128u + sw;
        const u32 rB0 = L_B2 + (u32)(gam * 9 + 4 * nh) * 1024u + l * 16, rB8 = L_B2 + (u32)(gam * 9 + 8) * 1024u + l * 16;
        f32x4 acc[4][4], acc8[2];
#pragma unroll
        for (int i = 0; i < 4; ++i)
#pragma unroll
            for (int t = 0; t < 4; ++t) acc[i][t] = (f32x4){0.f, 0.f, 0.f, 0.f};
        acc8[0] = acc8[1] = (f32x4){0.f, 0.f, 0.f, 0.f};
        bf16x8 fa[2][4], fb[2][5];
        auto step = [&](const int set, int i) {
            if (i <= 30) WAIT_V(10); else WAIT_V(0);
            WAIT_L0(); BAR(); SB();
            if (i == 15) {
                rA01 = (u32)((4 * mh + 2 * nh) * 2 + gam) * 1024u + l * 16; rA23 = (u32)((4 * mh + 2 * (1 - nh)) * 2 + gam) * 1024u + l * 16;
            }
            const u32 sl = ((i < 33) ? i + 1 : i) & 3;
            const u32 a01 = sl * 16384u + rA01, a23 = sl * 16384u + rA23, b0a = sl * 18432u + rB0, b8a = sl * 18432u + rB8;
            acc[0][0] = mfma(fb[set][0], fa[set][0], acc[0][0]); acc[1][0] = mfma(fb[set][0], fa[set][1], acc[1][0]); SB();
            if (i == 12) {
                const char* fp = (l < 8) ? ws + WS_FH + ((xcd * 4 + (mb >> 1)) * 8 + l) * 4 : ws + WS_FW + ((kap * 4 + nb) * 4 + (l & 3)) * 4;
                STAMP(8);
                poll(fp, l < 6 || (l >= 8 && l < 12), tag);
                STAMP(9);
            }
            if (i < 30) issueA(i + 4);
            SB();
            acc[2][0] = mfma(fb[set][0], fa[set][2], acc[2][0]); acc[3][0] = mfma(fb[set][0], fa[set][3], acc[3][0]); SB();
            if (i < 30) issueB(i + 4);
            SB();
#pragma unroll
            for (int m = 4; m < 18; ++m) {
                const int t = m >> 2, ii = m & 3, rr = m - 4;
                if (t < 4) acc[ii][t & 3] = mfma(fb[set][t], fa[set][ii], acc[ii][t & 3]); else acc8[ii & 1] = mfma(fb[set][4], fa[set][ii], acc8[ii & 1]);
                if (rr < 2) DSR128(fa[set ^ 1][rr], a01, 2048 * rr);
                else if (rr < 4) DSR128(fa[set ^ 1][rr], a23, 2048 * (rr - 2));
                else if (rr < 8) DSR128(fb[set ^ 1][rr - 4], b0a, 1024 * (rr - 4));
                else if (rr == 8) DSR128(fb[set ^ 1][4], b8a, 0);
                SB();
            }
        };
        WAIT_V(9); BAR();
        STAMP(7);
        DSR128(fa[0][0], rA01, 0); DSR128(fa[0][1], rA01, 2048); DSR128(fa[0][2], rA23, 0); DSR128(fa[0][3], rA23, 2048);
#pragma unroll
        for (int t = 0; t < 4; ++t) DSR128(fb[0][t], rB0, 1024 * t);
        DSR128(fb[0][4], rB8, 0);
#pragma unroll 1
        for (int i = 0; i < 34; i += 2) { step(0, i); step(1, i + 1); }
        asm volatile("s_waitcnt lgkmcnt(0)" :: "v"(fa[0][0]), "v"(fa[0][1]), "v"(fa[0][2]), "v"(fa[0][3]), "v"(fb[0][0]), "v"(fb[0][1]), "v"(fb[0][2]), "v"(fb[0][3]), "v"(fb[0][4]) : "memory");

        STAMP(10);
        const int wp = w & 3;
        const int tile = (mu * 8 + mb) * 4 + nb;
        auto blk = [&](int b) -> u32x4 {
            f32x4 lo = (b < 8) ? acc[2 * (b >> 2)][b & 3] : acc8[0], hi = (b < 8) ? acc[2 * (b >> 2) + 1][b & 3] : acc8[1];
            return (u32x4){pk2(lo[0], lo[1]), pk2(lo[2], lo[3]), pk2(hi[0], hi[1]), pk2(hi[2], hi[3])};
        };
        BAR();
        LAS u32x4* xb = (LAS u32x4*)((LAS char*)smem + (u32)(w * 9) * 1024u + l * 16);
        const bool own = (wp == kap);
#pragma unroll
        for (int b = 0; b < 9; ++b) {
            bool mine = (gam == 0) ? (b < 5) : (b >= 5);
            if (own || !mine) xb[b * 64] = blk(b);
        }
        WAIT_L0(); BAR();
        if (!own) {
            LAS u32x4* pb = (LAS u32x4*)((LAS char*)smem + (u32)((w ^ 4) * 9) * 1024u + l * 16);
            const u32 pso = WS_P + (u32)((tile * 36 + wp * 9) * 4 + kap) * 1024u;
#pragma unroll
            for (int b = 0; b < 9; ++b) {
                bool mine = (gam == 0) ? (b < 5) : (b >= 5);
                if (mine) {
                    u32x4 o = pb[b * 64];
                    f32x4 lo = (b < 8) ? acc[2 * (b >> 2)][b & 3] : acc8[0], hi = (b < 8) ? acc[2 * (b >> 2) + 1][b & 3] : acc8[1];
                    u32x4 s = {pk2(lo[0] + bflo(o[0]), lo[1] + bfhi(o[0])), pk2(lo[2] + bflo(o[1]), lo[3] + bfhi(o[1])),
                               pk2(hi[0] + bflo(o[2]), hi[1] + bfhi(o[2])), pk2(hi[2] + bflo(o[3]), hi[3] + bfhi(o[3]))};
                    __builtin_amdgcn_raw_buffer_store_b128(s, rs_ws, l * 16 + pso + b * 4096u, 0, 16);
                }
            }
        }
        WAIT_V(0); BAR();
        STAMP(11);
        if (tid == 0) st_flag(ws + WS_FP + (tile * 4 + kap) * 4, tag);
        poll(ws + WS_FP + (tile * 4 + (l & 3)) * 4, l < 4, tag);
        STAMP(12);
        const int omh = kap >> 1, onh = kap & 1;
#pragma unroll 1
        for (int b = w; b < 9; b += 8) {
            u32x4 p[3];
#pragma unroll
            for (int k = 0; k < 3; ++k) {
                int kk = (kap + 1 + k) & 3;
                p[k] = __builtin_amdgcn_raw_buffer_load_b128(rs_ws, l * 16, WS_P + (u32)((tile * 36 + kap * 9 + b) * 4 + kk) * 1024u, 16);
            }
            int P = (b < 8) ? (b >> 2) : 0, nf = (b < 8) ? 4 * onh + (b & 3) : 8;
            int trow = t2 + 64 * omh + 32 * ((P + onh) & 1) + r16, col = 144 * nb + 16 * nf + 4 * q;
            u32x2 x0 = *(const u32x2*)(x + (size_t)trow * D_ + col), x1 = *(const u32x2*)(x + (size_t)(trow + 16) * D_ + col);
            u32x4 o0 = *(LAS u32x4*)((LAS char*)smem + (u32)(kap * 9 + b) * 1024u + l * 16);
            u32x4 o1 = *(LAS u32x4*)((LAS char*)smem + (u32)((kap + 4) * 9 + b) * 1024u + l * 16);
            u32x4 xx = {x0[0], x0[1], x1[0], x1[1]};
            u32 rr[4];
#pragma unroll
            for (int d = 0; d < 4; ++d) {
                float lo = ((bflo(o0[d]) + bflo(o1[d])) + (bflo(p[0][d]) + bflo(p[1][d]))) + (bflo(p[2][d]) + bflo(xx[d]));
                float hi = ((bfhi(o0[d]) + bfhi(o1[d])) + (bfhi(p[0][d]) + bfhi(p[1][d]))) + (bfhi(p[2][d]) + bfhi(xx[d]));
                rr[d] = pk2(lo, hi);
            }
            *(u32x2*)(out + (size_t)trow * D_ + col) = (u32x2){rr[0], rr[1]};
            *(u32x2*)(out + (size_t)(trow + 16) * D_ + col) = (u32x2){rr[2], rr[3]};
        }
        WAIT_V(0); STAMP(13);
        if (bid == PW && w == 0) {
            unsigned long long te = __builtin_amdgcn_s_memrealtime() + 4 * (tb_ - ta_);
            for (int it = 0; it < 1000000; ++it) { if (__builtin_amdgcn_s_memrealtime() >= te) break; __builtin_amdgcn_s_sleep(1); }
        }
    }
}

extern "C" void kernel_launch(void* const* d_in, const int* in_sizes, int n_in, void* d_out, int out_size, void* d_ws, size_t ws_size,
                              hipStream_t stream) {
    (void)hipFuncSetAttribute((const void*)fused_kernel, hipFuncAttributeMaxDynamicSharedMemorySize, LDS_BYTES);
    hipLaunchKernelGGL(fused_kernel, dim3(256), dim3(512), LDS_BYTES, stream, (const u16*)d_in[0], (const u16*)d_in[1], (const u16*)d_in[2],
                       (const u16*)d_in[3], (const u16*)d_in[4], (const u16*)d_in[6], (const u16*)d_in[7], (u16*)d_out, (char*)d_ws);
}
```

```cpp
#include <hip/hip_runtime.h>
typedef unsigned short u16;
typedef unsigned u32;
typedef short bf16x8 __attribute__((ext_vector_type(8)));
typedef short s16x4 __attribute__((ext_vector_type(4)));
typedef __bf16 bf16x2 __attribute__((ext_vector_type(2)));
typedef float f32x4 __attribute__((ext_vector_type(4)));
typedef float f32x2 __attribute__((ext_vector_type(2)));
typedef unsigned u32x4 __attribute__((ext_vector_type(4)));
typedef unsigned u32x2 __attribute__((ext_vector_type(2)));
#define LAS __attribute__((address_space(3)))
#define DEVI __device__ __forceinline__

constexpr int S_ = 2048, D_ = 576, F_ = 4608;
constexpr u32 WS_EPOCH = 0;
constexpr u32 WS_FAB = 256;
constexpr u32 WS_FW = 1024;
constexpr u32 WS_FH = 2048;
constexpr u32 WS_FP = 4096;
constexpr u32 WS_STAT = 8192;
constexpr u32 WS_BM = 1u << 20;
constexpr u32 WS_H = 12u << 20;
constexpr u32 WS_P = 32u << 20;
constexpr u32 WS_END = 42u << 20;
constexpr u32 L_B1 = 96 * 1024;
constexpr u32 L_B2 = 64 * 1024;
constexpr u32 L_STAT = 150 * 1024;
constexpr u32 L_CS = 152 * 1024;
constexpr u32 LDS_BYTES = 153 * 1024;
constexpr int SPIN_MAX = 1 << 20;

extern __shared__ __attribute__((aligned(16))) char smem[];

#define WAIT_V(n) asm volatile("s_waitcnt vmcnt(" #n ")" ::: "memory")
#define WAIT_L0() asm volatile("s_waitcnt lgkmcnt(0)" ::: "memory")
#define BAR() __builtin_amdgcn_s_barrier()
#define STAMP(i)

#define SB() __builtin_amdgcn_sched_barrier(0)
#define DSR128(dst, addr, off) asm volatile("ds_read_b128 %0, %1 offset:%2" : "=v"(dst) : "v"(addr), "i"(off) : "memory")
#define DSRTR(dst, addr, off) asm volatile("ds_read_b64_tr_b16 %0, %1 offset:%2" : "=v"(dst) : "v"(addr), "i"(off) : "memory")
#define CAT(lo, hi) __builtin_shufflevector(lo, hi, 0, 1, 2, 3, 4, 5, 6, 7)
typedef __amdgpu_buffer_rsrc_t rsrc_t;
DEVI rsrc_t mk_rsrc(const void* p, u32 bytes) { return __builtin_amdgcn_make_buffer_rsrc((void*)p, 0, bytes, 0x00020000); }
template <int AUX> DEVI void dma(rsrc_t rs, u32 lds, u32 voff, u32 soff) {
    __builtin_amdgcn_raw_ptr_buffer_load_lds(rs, (LAS void*)((LAS char*)smem + lds), 16, voff, soff, 0, AUX);
}
DEVI bf16x8 lds128(u32 a) { return *(LAS bf16x8*)((LAS char*)smem + a); }
DEVI bf16x8 ldstr(u32 a0, u32 a1) {
    s16x4 lo = __builtin_amdgcn_ds_read_tr16_b64_v4i16((LAS s16x4*)((LAS char*)smem + a0));
    s16x4 hi = __builtin_amdgcn_ds_read_tr16_b64_v4i16((LAS s16x4*)((LAS char*)smem + a1));
    return __builtin_shufflevector(lo, hi, 0, 1, 2, 3, 4, 5, 6, 7);
}
DEVI f32x4 mfma(bf16x8 a, bf16x8 b, f32x4 c) { return __builtin_amdgcn_mfma_f32_16x16x32_bf16(a, b, c, 0, 0, 0); }
DEVI u32 pk2(float a, float b) { bf16x2 v = {(__bf16)a, (__bf16)b}; return __builtin_bit_cast(u32, v); }
DEVI float bflo(u32 v) { return __uint_as_float(v << 16); }
DEVI float bfhi(u32 v) { return __uint_as_float(v & 0xffff0000u); }
DEVI float dot1(u32 v, float acc) {
    return __builtin_amdgcn_fdot2_f32_bf16(__builtin_bit_cast(bf16x2, v), __builtin_bit_cast(bf16x2, 0x3f803f80u), acc, false);
}
#define DOT1(acc, v) acc = dot1(v, acc)
#define DOT4(acc, v) do { DOT1(acc, v[0]); DOT1(acc, v[1]); DOT1(acc, v[2]); DOT1(acc, v[3]); } while (0)
template <int CTRL> DEVI float dpp_add(float v) {
    return v + __builtin_bit_cast(float, __builtin_amdgcn_update_dpp(0, __builtin_bit_cast(int, v), CTRL, 0xf, 0xf, true));
}
DEVI float wave_sum(float v) {
    v = dpp_add<0xB1>(v); v = dpp_add<0x4E>(v); v = dpp_add<0x141>(v); v = dpp_add<0x140>(v);
    int iv = __builtin_bit_cast(int, v);
    float a = __builtin_bit_cast(float, __builtin_amdgcn_readlane(iv, 0)), b = __builtin_bit_cast(float, __builtin_amdgcn_readlane(iv, 16));
    float c = __builtin_bit_cast(float, __builtin_amdgcn_readlane(iv, 32)), d = __builtin_bit_cast(float, __builtin_amdgcn_readlane(iv, 48));
    return (a + b) + (c + d);
}
DEVI u32 ld_flag(const char* p) { return __hip_atomic_load((const u32*)p, __ATOMIC_RELAXED, __HIP_MEMORY_SCOPE_AGENT); }
DEVI void st_flag(char* p, u32 v) { __hip_atomic_store((u32*)p, v, __ATOMIC_RELAXED, __HIP_MEMORY_SCOPE_AGENT); }
DEVI void poll(const char* p, bool act, u32 tag) {
    for (int it = 0; it < SPIN_MAX; ++it) {
        u32 f = tag;
        if (act) f = ld_flag(p);
        if (__all(f == tag)) break;
        __builtin_amdgcn_s_sleep(2);
    }
}

__global__ __launch_bounds__(512, 2) void fused_kernel(const u16* __restrict__ x, const u16* __restrict__ Cr, const u16* __restrict__ Ci,
                                                       const u16* __restrict__ Dr, const u16* __restrict__ Di, const u16* __restrict__ w1,
                                                       const u16* __restrict__ w2, u16* __restrict__ out, char* __restrict__ ws) {
    const int tid = threadIdx.x, l = tid & 63, w = __builtin_amdgcn_readfirstlane(tid >> 6);
    const int bid = blockIdx.x, xcd = bid & 7, g = bid >> 3, kap = xcd >> 1, mu = xcd & 1;
    const int q = l >> 4, r16 = l & 15;
    const bool is_ab = g < 8;
    const rsrc_t rs_ws = mk_rsrc(ws, WS_END);
    const rsrc_t rs_x = mk_rsrc(x, S_ * D_ * 2);
    const rsrc_t rs_c = mk_rsrc((kap >> 1) ? Ci : Cr, S_ * S_ * 2);

    STAMP(0);
    u32 epoch; u32x4 rowv; u32 rowt;
    {
        const u16* xr = x + (size_t)(bid * 8 + w) * D_;
        asm volatile("global_load_dword %0, %1, off sc1" : "=v"(epoch) : "v"(ws + WS_EPOCH) : "memory");
        asm volatile("global_load_dwordx4 %0, %1, off" : "=v"(rowv) : "v"(xr + l * 8) : "memory");
        asm volatile("global_load_ushort %0, %1, off" : "=v"(rowt) : "v"(xr + 512 + l) : "memory");
    }

    const int mb = g >> 2, nb = g & 3;
    const int t2 = 1024 * mu + 128 * mb;
    const u32 vC0 = (u32)(16 * w + (l >> 3)) * 4096u + 16u * ((l & 7) ^ q);
    const u32 vC1 = (u32)(16 * w + 8 + (l >> 3)) * 4096u + 16u * ((l & 7) ^ (4 + q));
    const u32 sC = (u32)t2 * 4096u + 1024u * (kap & 1);

    const int wm = w & 3, wn = w >> 2;
    u32 tag;
    int row0;

    if (!is_ab) {
        const int gg = g - 8, rb = gg / 6, cb = gg - rb * 6;
        row0 = 1024 * mu + 256 * rb;
        const int f0 = 1152 * kap + 192 * cb;
        const rsrc_t rs_w = mk_rsrc(w1, D_ * F_ * 2);
        u32 vA0 = (u32)(16 * w + (l >> 3)) * 1152u + 16u * ((l & 7) ^ q);
        u32 vA1 = (u32)(16 * w + 8 + (l >> 3)) * 1152u + 16u * ((l & 7) ^ (4 + q));
        const u32 sA = (u32)row0 * 1152u;
        u32 vB0, vB1;
        {
            auto boff = [&](int p, int ll) -> u32 {
                int cg = p >> 2, g4 = p & 3, a8 = ll >> 3, c8 = ll & 7;
                int k = 16 * (g4 >> 1) + 8 * (a8 >> 2) + 4 * (g4 & 1) + (a8 & 3);
                int jj = (c8 >> 1) ^ ((a8 >> 1) & 3);
                return (u32)k * 9216u + (u32)(f0 * 2 + cg * 128 + 32 * jj + 16 * (c8 & 1));
            };
            vB0 = boff(w, l);
            vB1 = boff(8 + (w >> 1), (l & 31) + 32 * (w & 1));
        }
        const u32 lB0 = L_B1 + w * 1024, lB1 = L_B1 + (8 + (w >> 1)) * 1024 + (w & 1) * 512;
        auto issueA = [&](int unit, int hf) {
            u32 ld = (unit % 3) * 32768u + (16 * hf + 2 * w) * 1024u, so = sA + hf * (128u * 1152u) + unit * 128u;
            dma<0>(rs_x, ld, vA0, so); dma<0>(rs_x, ld + 1024, vA1, so);
        };
        auto issueB = [&](int u) {
            u32 so = (u32)u * (32u * 9216u), lo = (u & 3) * 12288u;
            dma<0>(rs_w, lB0 + lo, vB0, so);
            if (l < 32) dma<0>(rs_w, lB1 + lo, vB1, so);
        };
        issueA(0, 0); issueA(0, 1); issueB(0); issueB(1); issueA(1, 0); issueA(1, 1); issueB(2); issueB(3); issueA(2, 0); issueA(2, 1);

        asm volatile("s_waitcnt vmcnt(20)" : "+v"(epoch), "+v"(rowv), "+v"(rowt) :: "memory");
        tag = __builtin_amdgcn_readfirstlane(epoch) + 1u;
        {
            float v[9];
#pragma unroll
            for (int i = 0; i < 4; ++i) { v[2 * i] = bflo(rowv[i]); v[2 * i + 1] = bfhi(rowv[i]); }
            v[8] = bflo(rowt);
            float s = 0.f;
#pragma unroll
            for (int i = 0; i < 9; ++i) s += v[i];
            float mean = wave_sum(s) * (1.f / D_), ss = 0.f;
#pragma unroll
            for (int i = 0; i < 9; ++i) { float d = v[i] - mean; ss += d * d; }
            float rstd = 1.0f / sqrtf(wave_sum(ss) * (1.f / D_) + 1e-5f);
            u32x4 gr = {__float_as_uint(mean), tag, __float_as_uint(rstd), tag};
            if (l == 0) __builtin_amdgcn_raw_buffer_store_b128(gr, rs_ws, WS_STAT + (bid * 8 + w) * 16, 0, 16);
        }

        const u32 rA = (u32)(64 * wm + r16) * 128u + 16u * (q ^ (r16 >> 1));
        u32 rB[6];
        {
            int a8 = 4 * (q & 1) + ((l >> 2) & 3);
#pragma unroll
            for (int t = 0; t < 6; ++t) {
                int j = 6 * wn + t;
                rB[t] = L_B1 + (j >> 2) * 4096 + (q >> 1) * 2048 + a8 * 128 + 32 * ((j & 3) ^ ((a8 >> 1) & 3)) + 8 * (l & 3);
            }
        }
        f32x4 acc[4][6];
#pragma unroll
        for (int i = 0; i < 4; ++i)
#pragma unroll
            for (int t = 0; t < 6; ++t) acc[i][t] = (f32x4){0.f, 0.f, 0.f, 0.f};
        float csA = 0.f, csB = 0.f;
        bf16x8 fa[2][4]; s16x4 fl[2][6], fh[2][6];
        auto step = [&](const int set, int u) {
            if (u < 15) WAIT_V(8); else WAIT_V(4);
            WAIT_L0(); BAR(); SB();
            bf16x8 fb[6];
#pragma unroll
            for (int t = 0; t < 6; ++t) fb[t] = CAT(fl[set][t], fh[set][t]);
            const int un = (u < 17) ? u + 1 : u;
            const u32 ab = ((un >> 1) % 3) * 32768u + (rA ^ ((un & 1) * 64u)), bo = (un & 3) * 12288u;
            acc[0][0] = mfma(fb[0], fa[set][0], acc[0][0]); acc[1][0] = mfma(fb[0], fa[set][1], acc[1][0]); SB();
            if (u >= 1 && u <= 12) issueA(((u - 1) >> 1) + 3, set ^ 1);
            else if (u >= 13 && u <= 16) {
                int j = u - 13; u32 ld = j * 16384u + 2 * w * 1024u, so = sC + 128u * j;
                dma<0>(rs_c, ld, vC0, so); dma<0>(rs_c, ld + 1024, vC1, so);
            }
            SB();
            acc[2][0] = mfma(fb[0], fa[set][2], acc[2][0]); acc[3][0] = mfma(fb[0], fa[set][3], acc[3][0]); SB();
            if (u <= 13) issueB(u + 4);
            SB();
#pragma unroll
            for (int m = 4; m < 24; ++m) {
                const int t = m >> 2, i = m & 3, rr = m - 4;
                acc[i][t] = mfma(fb[t], fa[set][i], acc[i][t]);
                if (rr < 4) DSR128(fa[set ^ 1][rr], ab, 2048 * rr);
                else if (rr < 16) { const int tt = (rr - 4) >> 1; u32 ad = rB[tt] + bo; if (rr & 1) DSRTR(fh[set ^ 1][tt], ad, 1024); else DSRTR(fl[set ^ 1][tt], ad, 0); }
                SB();
            }
            const u32x4 b0 = __builtin_bit_cast(u32x4, fb[0]), b1 = __builtin_bit_cast(u32x4, fb[1]), b2 = __builtin_bit_cast(u32x4, fb[2]);
            const u32x4 b3 = __builtin_bit_cast(u32x4, fb[3]), b4 = __builtin_bit_cast(u32x4, fb[4]), b5 = __builtin_bit_cast(u32x4, fb[5]);
            if (wm == 0) { DOT4(csA, b0); DOT1(csB, b1[0]); DOT1(csB, b1[1]); }
            else if (wm == 1) { DOT4(csA, b2); DOT1(csB, b1[2]); DOT1(csB, b1[3]); }
            else if (wm == 2) { DOT4(csA, b3); DOT1(csB, b4[0]); DOT1(csB, b4[1]); }
            else { DOT4(csA, b5); DOT1(csB, b4[2]); DOT1(csB, b4[3]); }
        };
        WAIT_V(15); BAR();
        STAMP(1);
#pragma unroll
        for (int i = 0; i < 4; ++i) DSR128(fa[0][i], rA, 2048 * i);
#pragma unroll
        for (int t = 0; t < 6; ++t) { DSRTR(fl[0][t], rB[t], 0); DSRTR(fh[0][t], rB[t], 1024); }
#pragma unroll 1
        for (int u = 0; u < 18; u += 2) { step(0, u); step(1, u + 1); }
        asm volatile("s_waitcnt lgkmcnt(0)" :: "v"(fa[0][0]), "v"(fa[0][1]), "v"(fa[0][2]), "v"(fa[0][3]), "v"(fl[0][0]), "v"(fl[0][1]), "v"(fl[0][2]), "v"(fl[0][3]),
                     "v"(fl[0][4]), "v"(fl[0][5]), "v"(fh[0][0]), "v"(fh[0][1]), "v"(fh[0][2]), "v"(fh[0][3]), "v"(fh[0][4]), "v"(fh[0][5]) : "memory");

        STAMP(2);
        {
            float a = csA, b = csB;
            a += __shfl_xor(a, 16); a += __shfl_xor(a, 32); b += __shfl_xor(b, 16); b += __shfl_xor(b, 32);
            int ta = (wm == 0) ? 0 : (wm == 1) ? 2 : (wm == 2) ? 3 : 5, tb = (wm < 2) ? 1 : 4;
            if (l < 16) {
                *(LAS float*)((LAS char*)smem + L_CS + (96 * wn + 16 * ta + l) * 4) = a;
                *(LAS float*)((LAS char*)smem + L_CS + ((wm & 1) ? 192 + 32 * wn + 16 * (wm >> 1) + l : 96 * wn + 16 * tb + l) * 4) = b;
            }
        }
        if (w < 4) {
            u32x4 gr;
            for (int it = 0; it < SPIN_MAX; ++it) {
                gr = __builtin_amdgcn_raw_buffer_load_b128(rs_ws, WS_STAT + (row0 + tid) * 16, 0, 16);
                if (__all(gr[1] == tag && gr[3] == tag)) break;
                __builtin_amdgcn_s_sleep(2);
            }
            *(LAS u32x2*)((LAS char*)smem + L_STAT + tid * 8) = (u32x2){gr[0], gr[2]};
        }
        WAIT_L0(); BAR();
        STAMP(3);
        {
            f32x4 cs[6];
#pragma unroll
            for (int t = 0; t < 6; ++t) cs[t] = *(LAS f32x4*)((LAS char*)smem + L_CS + (96 * wn + 16 * t + 4 * q) * 4);
            cs[1] += *(LAS f32x4*)((LAS char*)smem + L_CS + (192 + 32 * wn + 4 * q) * 4);
            cs[4] += *(LAS f32x4*)((LAS char*)smem + L_CS + (192 + 32 * wn + 16 + 4 * q) * 4);
            const u32 hb = WS_H + (u32)(((row0 + 64 * wm) >> 7) * 72) * 16384u + l * 16;
#pragma unroll
            for (int i = 0; i < 4; ++i) {
                f32x2 st = *(LAS f32x2*)((LAS char*)smem + L_STAT + (64 * wm + 16 * i + r16) * 8);
#pragma unroll
                for (int P = 0; P < 3; ++P) {
                    float v[8];
#pragma unroll
                    for (int e = 0; e < 4; ++e) {
                        v[e] = fmaxf((acc[i][2 * P][e] - st[0] * cs[2 * P][e]) * st[1], 0.f);
                        v[4 + e] = fmaxf((acc[i][2 * P + 1][e] - st[0] * cs[2 * P + 1][e]) * st[1], 0.f);
                    }
                    u32x4 o = {pk2(v[0], v[1]), pk2(v[2], v[3]), pk2(v[4], v[5]), pk2(v[6], v[7])};
                    int k32 = (f0 + 96 * wn) / 32 + P;
                    u32 so = (u32)(k32 >> 1) * 16384u + (u32)((4 * (wm & 1) + i) * 2 + (k32 & 1)) * 1024u;
                    __builtin_amdgcn_raw_buffer_store_b128(o, rs_ws, hb + so, 0, 16);
                }
            }
        }
        WAIT_V(0); BAR();
        if (tid == 0) st_flag(ws + WS_FH + ((xcd * 4 + rb) * 8 + cb) * 4, tag);
        STAMP(4); STAMP(5);
    } else {
        const int mat = kap >> 1;
        row0 = 1024 * mu + 512 * (kap & 1) + 256 * (g >> 2);
        const rsrc_t rs_d = mk_rsrc(mat ? Di : Dr, D_ * D_ * 2);
        const int rperm = 32 * (w >> 1) + 4 * (w & 1) + 8 * (l >> 5) + ((l >> 3) & 3);
        u32 vA0 = (u32)rperm * 1152u + 16u * ((l & 7) ^ q);
        u32 vA1 = (u32)(rperm + 16) * 1152u + 16u * ((l & 7) ^ (4 + q));
        const u32 sA = (u32)row0 * 1152u;
        u32 vB[2], vBs[2];
#pragma unroll
        for (int hf = 0; hf < 2; ++hf) {
            int p = 9 * hf + w, rho = 8 * p + (l >> 3);
            vB[hf] = (u32)(144 * nb + rho) * 1152u + 16u * ((l & 7) ^ ((rho >> 1) & 7));
            int rho2 = 8 * (9 * hf + 8) + w;
            vBs[hf] = (u32)(144 * nb + rho2) * 1152u + 16u * ((l & 7) ^ ((rho2 >> 1) & 7));
        }
        auto issueA = [&](int unit, int hf) {
            u32 ld = (unit % 3) * 32768u + (16 * hf + 2 * w) * 1024u, so = sA + hf * (128u * 1152u) + unit * 128u;
            dma<0>(rs_x, ld, vA0, so); dma<0>(rs_x, ld + 1024, vA1, so);
        };
        auto issueB = [&](int unit, int hf) {
            u32 lb = L_B1 + (unit % 3) * 18432u + 9216u * hf, so = unit * 128u;
            dma<0>(rs_d, lb + w * 1024, hf ? vB[1] : vB[0], so);
            if (l < 8) dma<0>(rs_d, lb + 8192 + w * 128, hf ? vBs[1] : vBs[0], so);
        };
#pragma unroll
        for (int un = 0; un < 3; ++un) { issueA(un, 0); issueA(un, 1); issueB(un, 0); issueB(un, 1); }

        asm volatile("s_waitcnt vmcnt(24)" : "+v"(epoch), "+v"(rowv), "+v"(rowt) :: "memory");
        tag = __builtin_amdgcn_readfirstlane(epoch) + 1u;
        {
            float v[9];
#pragma unroll
            for (int i = 0; i < 4; ++i) { v[2 * i] = bflo(rowv[i]); v[2 * i + 1] = bfhi(rowv[i]); }
            v[8] = bflo(rowt);
            float s = 0.f;
#pragma unroll
            for (int i = 0; i < 9; ++i) s += v[i];
            float mean = wave_sum(s) * (1.f / D_), ss = 0.f;
#pragma unroll
            for (int i = 0; i < 9; ++i) { float d = v[i] - mean; ss += d * d; }
            float rstd = 1.0f / sqrtf(wave_sum(ss) * (1.f / D_) + 1e-5f);
            u32x4 gr = {__float_as_uint(mean), tag, __float_as_uint(rstd), tag};
            if (l == 0) __builtin_amdgcn_raw_buffer_store_b128(gr, rs_ws, WS_STAT + (bid * 8 + w) * 16, 0, 16);
        }

        const u32 sw = 16u * (q ^ (r16 >> 1));
        const u32 rA01 = (u32)(64 * wm + 32 * wn + r16) * 128u + sw, rA23 = (u32)(64 * wm + 32 * (1 - wn) + r16) * 128u + sw;
        const u32 rB0 = L_B1 + (u32)(64 * wn + r16) * 128u + sw, rB8 = L_B1 + (u32)(128 + r16) * 128u + sw;
        f32x4 acc[4][4], acc8[2];
#pragma unroll
        for (int i = 0; i < 4; ++i)
#pragma unroll
            for (int t = 0; t < 4; ++t) acc[i][t] = (f32x4){0.f, 0.f, 0.f, 0.f};
        acc8[0] = acc8[1] = (f32x4){0.f, 0.f, 0.f, 0.f};
        float csA = 0.f, csB = 0.f;
        bf16x8 fa[2][4], fb[2][5];
        auto step = [&](const int set, int u) {
            if (u < 15) WAIT_V(8); else WAIT_V(4);
            WAIT_L0(); BAR(); SB();
            const int un = (u < 17) ? u + 1 : u;
            const u32 sl = (un >> 1) % 3, xs = (un & 1) * 64u;
            const u32 a01 = sl * 32768u + (rA01 ^ xs), a23 = sl * 32768u + (rA23 ^ xs), b0a = sl * 18432u + (rB0 ^ xs), b8a = sl * 18432u + (rB8 ^ xs);
            acc[0][0] = mfma(fa[set][0], fb[set][0], acc[0][0]); acc[1][0] = mfma(fa[set][1], fb[set][0], acc[1][0]); SB();
            if (u >= 1 && u <= 12) { issueA(((u - 1) >> 1) + 3, set ^ 1); issueB(((u - 1) >> 1) + 3, set ^ 1); }
            else if (u >= 13 && u <= 16) {
                int j = u - 13; u32 ld = j * 16384u + 2 * w * 1024u, so = sC + 128u * j;
                dma<0>(rs_c, ld, vC0, so); dma<0>(rs_c, ld + 1024, vC1, so);
            }
            SB();
#pragma unroll
            for (int m = 2; m < 18; ++m) {
                const int t = m >> 2, i = m & 3, rr = m - 2;
                if (t < 4) acc[i][t & 3] = mfma(fa[set][i], fb[set][t], acc[i][t & 3]); else acc8[i & 1] = mfma(fa[set][i], fb[set][4], acc8[i & 1]);
                if (rr < 2) DSR128(fa[set ^ 1][rr], a01, 2048 * rr);
                else if (rr < 4) DSR128(fa[set ^ 1][rr], a23, 2048 * (rr - 2));
                else if (rr < 8) DSR128(fb[set ^ 1][rr - 4], b0a, 2048 * (rr - 4));
                else if (rr == 8) DSR128(fb[set ^ 1][4], b8a, 0);
                SB();
            }
            const u32x4 b0 = __builtin_bit_cast(u32x4, fb[set][0]), b1 = __builtin_bit_cast(u32x4, fb[set][1]), b2 = __builtin_bit_cast(u32x4, fb[set][2]);
            const u32x4 b3 = __builtin_bit_cast(u32x4, fb[set][3]), b4 = __builtin_bit_cast(u32x4, fb[set][4]);
            if (wm == 0) { DOT4(csA, b0); DOT1(csB, b4[0]); }
            else if (wm == 1) { DOT4(csA, b1); DOT1(csB, b4[1]); }
            else if (wm == 2) { DOT4(csA, b2); DOT1(csB, b4[2]); }
            else { DOT4(csA, b3); DOT1(csB, b4[3]); }
        };
        WAIT_V(17); BAR();
        STAMP(1);
        DSR128(fa[0][0], rA01, 0); DSR128(fa[0][1], rA01, 2048); DSR128(fa[0][2], rA23, 0); DSR128(fa[0][3], rA23, 2048);
#pragma unroll
        for (int t = 0; t < 4; ++t) DSR128(fb[0][t], rB0, 2048 * t);
        DSR128(fb[0][4], rB8, 0);
#pragma unroll 1
        for (int u = 0; u < 18; u += 2) { step(0, u); step(1, u + 1); }
        asm volatile("s_waitcnt lgkmcnt(0)" :: "v"(fa[0][0]), "v"(fa[0][1]), "v"(fa[0][2]), "v"(fa[0][3]), "v"(fb[0][0]), "v"(fb[0][1]), "v"(fb[0][2]), "v"(fb[0][3]), "v"(fb[0][4]) : "memory");
        BAR();

        STAMP(2);
        const int qd = 2 * mu + (g >> 2);
        {
            const rsrc_t rs_w2 = mk_rsrc(w2, F_ * D_ * 2);
            const u32 so = (u32)(1152 * kap + 288 * qd) * 1152u + 288u * nb;
#pragma unroll 1
            for (int p = w; p < 81; p += 8) {
                u32 o = p * 1024 + l * 16, row = o / 288u, ch = o - row * 288u;
                dma<0>(rs_w2, L_B2 + p * 1024, row * 1152u + ch, so);
            }
        }
        {
            float a = csA, b = csB;
            a += __shfl_xor(a, 16); a += __shfl_xor(a, 32); b += __shfl_xor(b, 16); b += __shfl_xor(b, 32);
            if (l < 16) {
                *(LAS float*)((LAS char*)smem + L_CS + (64 * wn + 16 * wm + l) * 4) = a;
                if (wn == 0) *(LAS float*)((LAS char*)smem + L_CS + (128 + 16 * wm + l) * 4) = b;
            }
        }
        if (w < 4) {
            u32x4 gr;
            for (int it = 0; it < SPIN_MAX; ++it) {
                gr = __builtin_amdgcn_raw_buffer_load_b128(rs_ws, WS_STAT + (row0 + tid) * 16, 0, 16);
                if (__all(gr[1] == tag && gr[3] == tag)) break;
                __builtin_amdgcn_s_sleep(2);
            }
            *(LAS u32x2*)((LAS char*)smem + L_STAT + tid * 8) = (u32x2){gr[0], gr[2]};
        }
        WAIT_L0(); BAR();
        STAMP(3);
        const u32 bmb = WS_BM + (u32)((kap * 4 + nb) * 68) * 9216u + l * 16;
        {
            float cs[5];
#pragma unroll
            for (int t = 0; t < 4; ++t) cs[t] = *(LAS float*)((LAS char*)smem + L_CS + (64 * wn + 16 * t + r16) * 4);
            {
                LAS float* c8 = (LAS float*)((LAS char*)smem + L_CS + (128 + r16) * 4);
                cs[4] = (c8[0] + c8[16]) + (c8[32] + c8[48]);
            }
            const float sgn = mat ? -1.f : 1.f;
#pragma unroll
            for (int Pi = 0; Pi < 2; ++Pi) {
                int sb = 64 * wm + 32 * ((Pi + wn) & 1);
                float mean[8], rs[8];
#pragma unroll
                for (int e = 0; e < 4; ++e) {
                    f32x4 st = *(LAS f32x4*)((LAS char*)smem + L_STAT + (sb + 8 * q + 2 * e) * 8);
                    mean[2 * e] = st[0]; rs[2 * e] = st[1] * sgn; mean[2 * e + 1] = st[2]; rs[2 * e + 1] = st[3] * sgn;
                }
                int kb = 16 * mu + 8 * (g >> 2) + (sb >> 5);
#pragma unroll
                for (int t = 0; t < 5; ++t) {
                    if (t == 4 && Pi == 1) break;
                    f32x4 lo = (t < 4) ? acc[2 * Pi][t & 3] : acc8[0], hi = (t < 4) ? acc[2 * Pi + 1][t & 3] : acc8[1];
                    float v[8];
#pragma unroll
                    for (int e = 0; e < 4; ++e) { v[e] = (lo[e] - mean[e] * cs[t]) * rs[e]; v[4 + e] = (hi[e] - mean[4 + e] * cs[t]) * rs[4 + e]; }
                    u32x4 o = {pk2(v[0], v[1]), pk2(v[2], v[3]), pk2(v[4], v[5]), pk2(v[6], v[7])};
                    int nf = (t < 4) ? 4 * wn + t : 8;
                    __builtin_amdgcn_raw_buffer_store_b128(o, rs_ws, bmb + (u32)(kb * 9 + nf) * 1024u, 0, 16);
                }
            }
        }
        WAIT_V(0); BAR();
        if (tid == 0) st_flag(ws + WS_FAB + ((kap * 4 + nb) * 4 + qd) * 4, tag);
        STAMP(4);
        {
            const u32 ra = L_B2 + (u32)(4 * q + ((l >> 2) & 3)) * 288u + 8u * (l & 3);
#pragma unroll 1
            for (int b = w; b < 81; b += 8) {
                int kb = b / 9, nf = b - kb * 9;
                u32 a0 = ra + kb * 9216 + nf * 32;
                bf16x8 v = ldstr(a0, a0 + 4608);
                __builtin_amdgcn_raw_buffer_store_b128(__builtin_bit_cast(u32x4, v), rs_ws, bmb + (u32)((32 + 9 * qd) * 9 + b) * 1024u, 0, 16);
            }
        }
        WAIT_V(0); WAIT_L0(); BAR();
        if (tid == 0) st_flag(ws + WS_FW + ((kap * 4 + nb) * 4 + qd) * 4, tag);
        STAMP(5);
    }

    if (bid == 255 && w == 7) {
#pragma unroll 1
        for (int c = 0; c < 4; ++c) poll(ws + WS_STAT + (size_t)(c * 64 + l) * 128 + 4, true, tag);
        if (l == 0) st_flag(ws + WS_EPOCH, tag);
    }

    {
        const int gam = w >> 2, mh = (w >> 1) & 1, nh = w & 1;
        const u32 sB = WS_BM + (u32)((kap * 4 + nb) * 68) * 9216u;
        const u32 vB = (u32)(2 * w) * 1024u + l * 16, vBq = (u32)(16 + (w >> 2)) * 1024u + (w & 3) * 256u + l * 16;
        auto issueB = [&](int j) {
            u32 lb = L_B2 + (j & 3) * 18432u, so = sB + (u32)j * 18432u;
            dma<16>(rs_ws, lb + 2 * w * 1024, vB, so); dma<16>(rs_ws, lb + 2 * w * 1024 + 1024, vB + 1024, so);
            if (l < 16) dma<16>(rs_ws, lb + (16 + (w >> 2)) * 1024 + (w & 3) * 256, vBq, so);
        };
        const u32 sH = WS_H + (u32)((t2 >> 7) * 72 + 18 * kap) * 16384u;
        auto issueA = [&](int j) {
            u32 ld = (j & 3) * 16384u + 2 * w * 1024u;
            if (j < 16) { u32 so = sC + 2048u * (j >> 3) + 128u * (j & 7); dma<0>(rs_c, ld, vC0, so); dma<0>(rs_c, ld + 1024, vC1, so); }
            else { u32 so = sH + (u32)(j - 16) * 16384u; dma<16>(rs_ws, ld, vB, so); dma<16>(rs_ws, ld + 1024, vB + 1024, so); }
        };
        poll(ws + WS_FAB + ((kap * 4 + nb) * 4 + (l & 3)) * 4, l < 4, tag);
        STAMP(6);
        issueB(0); issueB(1); issueB(2); issueB(3);

        const u32 sw = 16u * ((4 * gam + q) ^ (r16 >> 1));
        u32 rA01 = (u32)(64 * mh + 32 * nh + r16) * 128u + sw, rA23 = (u32)(64 * mh + 32 * (1 - nh) + r16) * 128u + sw;
        const u32 rB0 = L_B2 + (u32)(gam * 9 + 4 * nh) * 1024u + l * 16, rB8 = L_B2 + (u32)(gam * 9 + 8) * 1024u + l * 16;
        f32x4 acc[4][4], acc8[2];
#pragma unroll
        for (int i = 0; i < 4; ++i)
#pragma unroll
            for (int t = 0; t < 4; ++t) acc[i][t] = (f32x4){0.f, 0.f, 0.f, 0.f};
        acc8[0] = acc8[1] = (f32x4){0.f, 0.f, 0.f, 0.f};
        bf16x8 fa[2][4], fb[2][5];
        auto step = [&](const int set, int i) {
            if (i <= 30) WAIT_V(10); else WAIT_V(0);
            WAIT_L0(); BAR(); SB();
            if (i == 15) {
                rA01 = (u32)((4 * mh + 2 * nh) * 2 + gam) * 1024u + l * 16; rA23 = (u32)((4 * mh + 2 * (1 - nh)) * 2 + gam) * 1024u + l * 16;
            }
            const u32 sl = ((i < 33) ? i + 1 : i) & 3;
            const u32 a01 = sl * 16384u + rA01, a23 = sl * 16384u + rA23, b0a = sl * 18432u + rB0, b8a = sl * 18432u + rB8;
            acc[0][0] = mfma(fb[set][0], fa[set][0], acc[0][0]); acc[1][0] = mfma(fb[set][0], fa[set][1], acc[1][0]); SB();
            if (i == 12) {
                const char* fp = (l < 8) ? ws + WS_FH + ((xcd * 4 + (mb >> 1)) * 8 + l) * 4 : ws + WS_FW + ((kap * 4 + nb) * 4 + (l & 3)) * 4;
                STAMP(8);
                poll(fp, l < 6 || (l >= 8 && l < 12), tag);
                STAMP(9);
            }
            if (i < 30) issueA(i + 4);
            SB();
            acc[2][0] = mfma(fb[set][0], fa[set][2], acc[2][0]); acc[3][0] = mfma(fb[set][0], fa[set][3], acc[3][0]); SB();
            if (i < 30) issueB(i + 4);
            SB();
#pragma unroll
            for (int m = 4; m < 18; ++m) {
                const int t = m >> 2, ii = m & 3, rr = m - 4;
                if (t < 4) acc[ii][t & 3] = mfma(fb[set][t], fa[set][ii], acc[ii][t & 3]); else acc8[ii & 1] = mfma(fb[set][4], fa[set][ii], acc8[ii & 1]);
                if (rr < 2) DSR128(fa[set ^ 1][rr], a01, 2048 * rr);
                else if (rr < 4) DSR128(fa[set ^ 1][rr], a23, 2048 * (rr - 2));
                else if (rr < 8) DSR128(fb[set ^ 1][rr - 4], b0a, 1024 * (rr - 4));
                else if (rr == 8) DSR128(fb[set ^ 1][4], b8a, 0);
                SB();
            }
        };
        WAIT_V(9); BAR();
        STAMP(7);
        DSR128(fa[0][0], rA01, 0); DSR128(fa[0][1], rA01, 2048); DSR128(fa[0][2], rA23, 0); DSR128(fa[0][3], rA23, 2048);
#pragma unroll
        for (int t = 0; t < 4; ++t) DSR128(fb[0][t], rB0, 1024 * t);
        DSR128(fb[0][4], rB8, 0);
#pragma unroll 1
        for (int i = 0; i < 34; i += 2) { step(0, i); step(1, i + 1); }
        asm volatile("s_waitcnt lgkmcnt(0)" :: "v"(fa[0][0]), "v"(fa[0][1]), "v"(fa[0][2]), "v"(fa[0][3]), "v"(fb[0][0]), "v"(fb[0][1]), "v"(fb[0][2]), "v"(fb[0][3]), "v"(fb[0][4]) : "memory");

        STAMP(10);
        const int wp = w & 3;
        const int tile = (mu * 8 + mb) * 4 + nb;
        auto blk = [&](int b) -> u32x4 {
            f32x4 lo = (b < 8) ? acc[2 * (b >> 2)][b & 3] : acc8[0], hi = (b < 8) ? acc[2 * (b >> 2) + 1][b & 3] : acc8[1];
            return (u32x4){pk2(lo[0], lo[1]), pk2(lo[2], lo[3]), pk2(hi[0], hi[1]), pk2(hi[2], hi[3])};
        };
        BAR();
        LAS u32x4* xb = (LAS u32x4*)((LAS char*)smem + (u32)(w * 9) * 1024u + l * 16);
        const bool own = (wp == kap);
#pragma unroll
        for (int b = 0; b < 9; ++b) {
            bool mine = (gam == 0) ? (b < 5) : (b >= 5);
            if (own || !mine) xb[b * 64] = blk(b);
        }
        WAIT_L0(); BAR();
        if (!own) {
            LAS u32x4* pb = (LAS u32x4*)((LAS char*)smem + (u32)((w ^ 4) * 9) * 1024u + l * 16);
            const u32 pso = WS_P + (u32)((tile * 36 + wp * 9) * 4 + kap) * 1024u;
#pragma unroll
            for (int b = 0; b < 9; ++b) {
                bool mine = (gam == 0) ? (b < 5) : (b >= 5);
                if (mine) {
                    u32x4 o = pb[b * 64];
                    f32x4 lo = (b < 8) ? acc[2 * (b >> 2)][b & 3] : acc8[0], hi = (b < 8) ? acc[2 * (b >> 2) + 1][b & 3] : acc8[1];
                    u32x4 s = {pk2(lo[0] + bflo(o[0]), lo[1] + bfhi(o[0])), pk2(lo[2] + bflo(o[1]), lo[3] + bfhi(o[1])),
                               pk2(hi[0] + bflo(o[2]), hi[1] + bfhi(o[2])), pk2(hi[2] + bflo(o[3]), hi[3] + bfhi(o[3]))};
                    __builtin_amdgcn_raw_buffer_store_b128(s, rs_ws, l * 16 + pso + b * 4096u, 0, 16);
                }
            }
        }
        WAIT_V(0); BAR();
        STAMP(11);
        if (tid == 0) st_flag(ws + WS_FP + (tile * 4 + kap) * 4, tag);
        poll(ws + WS_FP + (tile * 4 + (l & 3)) * 4, l < 4, tag);
        STAMP(12);
        const int omh = kap >> 1, onh = kap & 1;
#pragma unroll 1
        for (int b = w; b < 9; b += 8) {
            u32x4 p[3];
#pragma unroll
            for (int k = 0; k < 3; ++k) {
                int kk = (kap + 1 + k) & 3;
                p[k] = __builtin_amdgcn_raw_buffer_load_b128(rs_ws, l * 16, WS_P + (u32)((tile * 36 + kap * 9 + b) * 4 + kk) * 1024u, 16);
            }
            int P = (b < 8) ? (b >> 2) : 0, nf = (b < 8) ? 4 * onh + (b & 3) : 8;
            int trow = t2 + 64 * omh + 32 * ((P + onh) & 1) + r16, col = 144 * nb + 16 * nf + 4 * q;
            u32x2 x0 = *(const u32x2*)(x + (size_t)trow * D_ + col), x1 = *(const u32x2*)(x + (size_t)(trow + 16) * D_ + col);
            u32x4 o0 = *(LAS u32x4*)((LAS char*)smem + (u32)(kap * 9 + b) * 1024u + l * 16);
            u32x4 o1 = *(LAS u32x4*)((LAS char*)smem + (u32)((kap + 4) * 9 + b) * 1024u + l * 16);
            u32x4 xx = {x0[0], x0[1], x1[0], x1[1]};
            u32 rr[4];
#pragma unroll
            for (int d = 0; d < 4; ++d) {
                float lo = ((bflo(o0[d]) + bflo(o1[d])) + (bflo(p[0][d]) + bflo(p[1][d]))) + (bflo(p[2][d]) + bflo(xx[d]));
                float hi = ((bfhi(o0[d]) + bfhi(o1[d])) + (bfhi(p[0][d]) + bfhi(p[1][d]))) + (bfhi(p[2][d]) + bfhi(xx[d]));
                rr[d] = pk2(lo, hi);
            }
            *(u32x2*)(out + (size_t)trow * D_ + col) = (u32x2){rr[0], rr[1]};
            *(u32x2*)(out + (size_t)(trow + 16) * D_ + col) = (u32x2){rr[2], rr[3]};
        }
        WAIT_V(0); STAMP(13);
    }
}

extern "C" void kernel_launch(void* const* d_in, const int* in_sizes, int n_in, void* d_out, int out_size, void* d_ws, size_t ws_size,
                              hipStream_t stream) {
    (void)hipFuncSetAttribute((const void*)fused_kernel, hipFuncAttributeMaxDynamicSharedMemorySize, LDS_BYTES);
    hipLaunchKernelGGL(fused_kernel, dim3(256), dim3(512), LDS_BYTES, stream, (const u16*)d_in[0], (const u16*)d_in[1], (const u16*)d_in[2],
                       (const u16*)d_in[3], (const u16*)d_in[4], (const u16*)d_in[6], (const u16*)d_in[7], (u16*)d_out, (char*)d_ws);
}
```

```cpp
#define OAUX 16
#include <hip/hip_runtime.h>
typedef unsigned short u16;
typedef unsigned u32;
typedef short bf16x8 __attribute__((ext_vector_type(8)));
typedef short s16x4 __attribute__((ext_vector_type(4)));
typedef __bf16 bf16x2 __attribute__((ext_vector_type(2)));
typedef float f32x4 __attribute__((ext_vector_type(4)));
typedef float f32x2 __attribute__((ext_vector_type(2)));
typedef unsigned u32x4 __attribute__((ext_vector_type(4)));
typedef unsigned u32x2 __attribute__((ext_vector_type(2)));
#define LAS __attribute__((address_space(3)))
#define DEVI __device__ __forceinline__

constexpr int S_ = 2048, D_ = 576, F_ = 4608;
constexpr u32 WS_EPOCH = 0;
constexpr u32 WS_FAB = 256;
constexpr u32 WS_FW = 1024;
constexpr u32 WS_FH = 2048;
constexpr u32 WS_FP = 4096;
constexpr u32 WS_STAT = 8192;
constexpr u32 WS_BM = 1u << 20;
constexpr u32 WS_H = 12u << 20;
constexpr u32 WS_P = 32u << 20;
constexpr u32 WS_END = 42u << 20;
constexpr u32 L_B1 = 96 * 1024;
constexpr u32 L_B2 = 64 * 1024;
constexpr u32 L_STAT = 150 * 1024;
constexpr u32 L_CS = 152 * 1024;
constexpr u32 LDS_BYTES = 153 * 1024;
constexpr int SPIN_MAX = 1 << 20;

extern __shared__ __attribute__((aligned(16))) char smem[];

#define WAIT_V(n) asm volatile("s_waitcnt vmcnt(" #n ")" ::: "memory")
#define WAIT_L0() asm volatile("s_waitcnt lgkmcnt(0)" ::: "memory")
#define BAR() __builtin_amdgcn_s_barrier()
#define STAMP(i)

#define SB() __builtin_amdgcn_sched_barrier(0)
#define DSR128(dst, addr, off) asm volatile("ds_read_b128 %0, %1 offset:%2" : "=v"(dst) : "v"(addr), "i"(off) : "memory")
#define DSRTR(dst, addr, off) asm volatile("ds_read_b64_tr_b16 %0, %1 offset:%2" : "=v"(dst) : "v"(addr), "i"(off) : "memory")
#define CAT(lo, hi) __builtin_shufflevector(lo, hi, 0, 1, 2, 3, 4, 5, 6, 7)
typedef __amdgpu_buffer_rsrc_t rsrc_t;
DEVI rsrc_t mk_rsrc(const void* p, u32 bytes) { return __builtin_amdgcn_make_buffer_rsrc((void*)p, 0, bytes, 0x00020000); }
template <int AUX> DEVI void dma(rsrc_t rs, u32 lds, u32 voff, u32 soff) {
    __builtin_amdgcn_raw_ptr_buffer_load_lds(rs, (LAS void*)((LAS char*)smem + lds), 16, voff, soff, 0, AUX);
}
DEVI bf16x8 lds128(u32 a) { return *(LAS bf16x8*)((LAS char*)smem + a); }
DEVI bf16x8 ldstr(u32 a0, u32 a1) {
    s16x4 lo = __builtin_amdgcn_ds_read_tr16_b64_v4i16((LAS s16x4*)((LAS char*)smem + a0));
    s16x4 hi = __builtin_amdgcn_ds_read_tr16_b64_v4i16((LAS s16x4*)((LAS char*)smem + a1));
    return __builtin_shufflevector(lo, hi, 0, 1, 2, 3, 4, 5, 6, 7);
}
DEVI f32x4 mfma(bf16x8 a, bf16x8 b, f32x4 c) { return __builtin_amdgcn_mfma_f32_16x16x32_bf16(a, b, c, 0, 0, 0); }
DEVI u32 pk2(float a, float b) { bf16x2 v = {(__bf16)a, (__bf16)b}; return __builtin_bit_cast(u32, v); }
DEVI float bflo(u32 v) { return __uint_as_float(v << 16); }
DEVI float bfhi(u32 v) { return __uint_as_float(v & 0xffff0000u); }
DEVI float dot1(u32 v, float acc) {
    return __builtin_amdgcn_fdot2_f32_bf16(__builtin_bit_cast(bf16x2, v), __builtin_bit_cast(bf16x2, 0x3f803f80u), acc, false);
}
#define DOT1(acc, v) acc = dot1(v, acc)
#define DOT4(acc, v) do { DOT1(acc, v[0]); DOT1(acc, v[1]); DOT1(acc, v[2]); DOT1(acc, v[3]); } while (0)
template <int CTRL> DEVI float dpp_add(float v) {
    return v + __builtin_bit_cast(float, __builtin_amdgcn_update_dpp(0, __builtin_bit_cast(int, v), CTRL, 0xf, 0xf, true));
}
DEVI float wave_sum(float v) {
    v = dpp_add<0xB1>(v); v = dpp_add<0x4E>(v); v = dpp_add<0x141>(v); v = dpp_add<0x140>(v);
    int iv = __builtin_bit_cast(int, v);
    float a = __builtin_bit_cast(float, __builtin_amdgcn_readlane(iv, 0)), b = __builtin_bit_cast(float, __builtin_amdgcn_readlane(iv, 16));
    float c = __builtin_bit_cast(float, __builtin_amdgcn_readlane(iv, 32)), d = __builtin_bit_cast(float, __builtin_amdgcn_readlane(iv, 48));
    return (a + b) + (c + d);
}
DEVI u32 ld_flag(const char* p) { return __hip_atomic_load((const u32*)p, __ATOMIC_RELAXED, __HIP_MEMORY_SCOPE_AGENT); }
DEVI void st_flag(char* p, u32 v) { __hip_atomic_store((u32*)p, v, __ATOMIC_RELAXED, __HIP_MEMORY_SCOPE_AGENT); }
DEVI void poll(const char* p, bool act, u32 tag) {
    for (int it = 0; it < SPIN_MAX; ++it) {
        u32 f = tag;
        if (act) f = ld_flag(p);
        if (__all(f == tag)) break;
        __builtin_amdgcn_s_sleep(2);
    }
}

__global__ __launch_bounds__(512, 2) void fused_kernel(const u16* __restrict__ x, const u16* __restrict__ Cr, const u16* __restrict__ Ci,
                                                       const u16* __restrict__ Dr, const u16* __restrict__ Di, const u16* __restrict__ w1,
                                                       const u16* __restrict__ w2, u16* __restrict__ out, char* __restrict__ ws) {
    const int tid = threadIdx.x, l = tid & 63, w = __builtin_amdgcn_readfirstlane(tid >> 6);
    const int bid = blockIdx.x, xcd = bid & 7, g = bid >> 3, kap = xcd >> 1, mu = xcd & 1;
    const int q = l >> 4, r16 = l & 15;
    const bool is_ab = g < 8;
    const rsrc_t rs_ws = mk_rsrc(ws, WS_END);
    const rsrc_t rs_x = mk_rsrc(x, S_ * D_ * 2);
    const rsrc_t rs_c = mk_rsrc((kap >> 1) ? Ci : Cr, S_ * S_ * 2);

    STAMP(0);
    u32 epoch; u32x4 rowv; u32 rowt;
    {
        const u16* xr = x + (size_t)(bid * 8 + w) * D_;
        asm volatile("global_load_dword %0, %1, off sc1" : "=v"(epoch) : "v"(ws + WS_EPOCH) : "memory");
        asm volatile("global_load_dwordx4 %0, %1, off" : "=v"(rowv) : "v"(xr + l * 8) : "memory");
        asm volatile("global_load_ushort %0, %1, off" : "=v"(rowt) : "v"(xr + 512 + l) : "memory");
    }

    const int mb = g >> 2, nb = g & 3;
    const int t2 = 1024 * mu + 128 * mb;
    const u32 vC0 = (u32)(16 * w + (l >> 3)) * 4096u + 16u * ((l & 7) ^ q);
    const u32 vC1 = (u32)(16 * w + 8 + (l >> 3)) * 4096u + 16u * ((l & 7) ^ (4 + q));
    const u32 sC = (u32)t2 * 4096u + 1024u * (kap & 1);

    const int wm = w & 3, wn = w >> 2;
    u32 tag;
    int row0;

    if (!is_ab) {
        const int gg = g - 8, rb = gg / 6, cb = gg - rb * 6;
        row0 = 1024 * mu + 256 * rb;
        const int f0 = 1152 * kap + 192 * cb;
        const rsrc_t rs_w = mk_rsrc(w1, D_ * F_ * 2);
        u32 vA0 = (u32)(16 * w + (l >> 3)) * 1152u + 16u * ((l & 7) ^ q);
        u32 vA1 = (u32)(16 * w + 8 + (l >> 3)) * 1152u + 16u * ((l & 7) ^ (4 + q));
        const u32 sA = (u32)row0 * 1152u;
        u32 vB0, vB1;
        {
            auto boff = [&](int p, int ll) -> u32 {
                int cg = p >> 2, g4 = p & 3, a8 = ll >> 3, c8 = ll & 7;
                int k = 16 * (g4 >> 1) + 8 * (a8 >> 2) + 4 * (g4 & 1) + (a8 & 3);
                int jj = (c8 >> 1) ^ ((a8 >> 1) & 3);
                return (u32)k * 9216u + (u32)(f0 * 2 + cg * 128 + 32 * jj + 16 * (c8 & 1));
            };
            vB0 = boff(w, l);
            vB1 = boff(8 + (w >> 1), (l & 31) + 32 * (w & 1));
        }
        const u32 lB0 = L_B1 + w * 1024, lB1 = L_B1 + (8 + (w >> 1)) * 1024 + (w & 1) * 512;
        auto issueA = [&](int unit, int hf) {
            u32 ld = (unit % 3) * 32768u + (16 * hf + 2 * w) * 1024u, so = sA + hf * (128u * 1152u) + unit * 128u;
            dma<0>(rs_x, ld, vA0, so); dma<0>(rs_x, ld + 1024, vA1, so);
        };
        auto issueB = [&](int u) {
            u32 so = (u32)u * (32u * 9216u), lo = (u & 3) * 12288u;
            dma<0>(rs_w, lB0 + lo, vB0, so);
            if (l < 32) dma<0>(rs_w, lB1 + lo, vB1, so);
        };
        issueA(0, 0); issueA(0, 1); issueB(0); issueB(1); issueA(1, 0); issueA(1, 1); issueB(2); issueB(3); issueA(2, 0); issueA(2, 1);

        asm volatile("s_waitcnt vmcnt(20)" : "+v"(epoch), "+v"(rowv), "+v"(rowt) :: "memory");
        tag = __builtin_amdgcn_readfirstlane(epoch) + 1u;
        {
            float v[9];
#pragma unroll
            for (int i = 0; i < 4; ++i) { v[2 * i] = bflo(rowv[i]); v[2 * i + 1] = bfhi(rowv[i]); }
            v[8] = bflo(rowt);
            float s = 0.f;
#pragma unroll
            for (int i = 0; i < 9; ++i) s += v[i];
            float mean = wave_sum(s) * (1.f / D_), ss = 0.f;
#pragma unroll
            for (int i = 0; i < 9; ++i) { float d = v[i] - mean; ss += d * d; }
            float rstd = 1.0f / sqrtf(wave_sum(ss) * (1.f / D_) + 1e-5f);
            u32x4 gr = {__float_as_uint(mean), tag, __float_as_uint(rstd), tag};
            if (l == 0) __builtin_amdgcn_raw_buffer_store_b128(gr, rs_ws, WS_STAT + (bid * 8 + w) * 16, 0, 16);
        }

        const u32 rA = (u32)(64 * wm + r16) * 128u + 16u * (q ^ (r16 >> 1));
        u32 rB[6];
        {
            int a8 = 4 * (q & 1) + ((l >> 2) & 3);
#pragma unroll
            for (int t = 0; t < 6; ++t) {
                int j = 6 * wn + t;
                rB[t] = L_B1 + (j >> 2) * 4096 + (q >> 1) * 2048 + a8 * 128 + 32 * ((j & 3) ^ ((a8 >> 1) & 3)) + 8 * (l & 3);
            }
        }
        f32x4 acc[4][6];
#pragma unroll
        for (int i = 0; i < 4; ++i)
#pragma unroll
            for (int t = 0; t < 6; ++t) acc[i][t] = (f32x4){0.f, 0.f, 0.f, 0.f};
        float csA = 0.f, csB = 0.f;
        bf16x8 fa[2][4]; s16x4 fl[2][6], fh[2][6];
        auto step = [&](const int set, int u) {
            if (u < 15) WAIT_V(8); else WAIT_V(4);
            WAIT_L0(); BAR(); SB();
            bf16x8 fb[6];
#pragma unroll
            for (int t = 0; t < 6; ++t) fb[t] = CAT(fl[set][t], fh[set][t]);
            const int un = (u < 17) ? u + 1 : u;
            const u32 ab = ((un >> 1) % 3) * 32768u + (rA ^ ((un & 1) * 64u)), bo = (un & 3) * 12288u;
            acc[0][0] = mfma(fb[0], fa[set][0], acc[0][0]); acc[1][0] = mfma(fb[0], fa[set][1], acc[1][0]); SB();
            if (u >= 1 && u <= 12) issueA(((u - 1) >> 1) + 3, set ^ 1);
            else if (u >= 13 && u <= 16) {
                int j = u - 13; u32 ld = j * 16384u + 2 * w * 1024u, so = sC + 128u * j;
                dma<0>(rs_c, ld, vC0, so); dma<0>(rs_c, ld + 1024, vC1, so);
            }
            SB();
            acc[2][0] = mfma(fb[0], fa[set][2], acc[2][0]); acc[3][0] = mfma(fb[0], fa[set][3], acc[3][0]); SB();
            if (u <= 13) issueB(u + 4);
            SB();
#pragma unroll
            for (int m = 4; m < 24; ++m) {
                const int t = m >> 2, i = m & 3, rr = m - 4;
                acc[i][t] = mfma(fb[t], fa[set][i], acc[i][t]);
                if (rr < 4) DSR128(fa[set ^ 1][rr], ab, 2048 * rr);
                else if (rr < 16) { const int tt = (rr - 4) >> 1; u32 ad = rB[tt] + bo; if (rr & 1) DSRTR(fh[set ^ 1][tt], ad, 1024); else DSRTR(fl[set ^ 1][tt], ad, 0); }
                SB();
            }
            const u32x4 b0 = __builtin_bit_cast(u32x4, fb[0]), b1 = __builtin_bit_cast(u32x4, fb[1]), b2 = __builtin_bit_cast(u32x4, fb[2]);
            const u32x4 b3 = __builtin_bit_cast(u32x4, fb[3]), b4 = __builtin_bit_cast(u32x4, fb[4]), b5 = __builtin_bit_cast(u32x4, fb[5]);
            if (wm == 0) { DOT4(csA, b0); DOT1(csB, b1[0]); DOT1(csB, b1[1]); }
            else if (wm == 1) { DOT4(csA, b2); DOT1(csB, b1[2]); DOT1(csB, b1[3]); }
            else if (wm == 2) { DOT4(csA, b3); DOT1(csB, b4[0]); DOT1(csB, b4[1]); }
            else { DOT4(csA, b5); DOT1(csB, b4[2]); DOT1(csB, b4[3]); }
        };
        WAIT_V(15); BAR();
        STAMP(1);
#pragma unroll
        for (int i = 0; i < 4; ++i) DSR128(fa[0][i], rA, 2048 * i);
#pragma unroll
        for (int t = 0; t < 6; ++t) { DSRTR(fl[0][t], rB[t], 0); DSRTR(fh[0][t], rB[t], 1024); }
#pragma unroll 1
        for (int u = 0; u < 18; u += 2) { step(0, u); step(1, u + 1); }
        asm volatile("s_waitcnt lgkmcnt(0)" :: "v"(fa[0][0]), "v"(fa[0][1]), "v"(fa[0][2]), "v"(fa[0][3]), "v"(fl[0][0]), "v"(fl[0][1]), "v"(fl[0][2]), "v"(fl[0][3]),
                     "v"(fl[0][4]), "v"(fl[0][5]), "v"(fh[0][0]), "v"(fh[0][1]), "v"(fh[0][2]), "v"(fh[0][3]), "v"(fh[0][4]), "v"(fh[0][5]) : "memory");

        STAMP(2);
        {
            float a = csA, b = csB;
            a += __shfl_xor(a, 16); a += __shfl_xor(a, 32); b += __shfl_xor(b, 16); b += __shfl_xor(b, 32);
            int ta = (wm == 0) ? 0 : (wm == 1) ? 2 : (wm == 2) ? 3 : 5, tb = (wm < 2) ? 1 : 4;
            if (l < 16) {
                *(LAS float*)((LAS char*)smem + L_CS + (96 * wn + 16 * ta + l) * 4) = a;
                *(LAS float*)((LAS char*)smem + L_CS + ((wm & 1) ? 192 + 32 * wn + 16 * (wm >> 1) + l : 96 * wn + 16 * tb + l) * 4) = b;
            }
        }
        if (w < 4) {
            u32x4 gr;
            for (int it = 0; it < SPIN_MAX; ++it) {
                gr = __builtin_amdgcn_raw_buffer_load_b128(rs_ws, WS_STAT + (row0 + tid) * 16, 0, 16);
                if (__all(gr[1] == tag && gr[3] == tag)) break;
                __builtin_amdgcn_s_sleep(2);
            }
            *(LAS u32x2*)((LAS char*)smem + L_STAT + tid * 8) = (u32x2){gr[0], gr[2]};
        }
        WAIT_L0(); BAR();
        STAMP(3);
        {
            f32x4 cs[6];
#pragma unroll
            for (int t = 0; t < 6; ++t) cs[t] = *(LAS f32x4*)((LAS char*)smem + L_CS + (96 * wn + 16 * t + 4 * q) * 4);
            cs[1] += *(LAS f32x4*)((LAS char*)smem + L_CS + (192 + 32 * wn + 4 * q) * 4);
            cs[4] += *(LAS f32x4*)((LAS char*)smem + L_CS + (192 + 32 * wn + 16 + 4 * q) * 4);
            const u32 hb = WS_H + (u32)(((row0 + 64 * wm) >> 7) * 72) * 16384u + l * 16;
#pragma unroll
            for (int i = 0; i < 4; ++i) {
                f32x2 st = *(LAS f32x2*)((LAS char*)smem + L_STAT + (64 * wm + 16 * i + r16) * 8);
#pragma unroll
                for (int P = 0; P < 3; ++P) {
                    float v[8];
#pragma unroll
                    for (int e = 0; e < 4; ++e) {
                        v[e] = fmaxf((acc[i][2 * P][e] - st[0] * cs[2 * P][e]) * st[1], 0.f);
                        v[4 + e] = fmaxf((acc[i][2 * P + 1][e] - st[0] * cs[2 * P + 1][e]) * st[1], 0.f);
                    }
                    u32x4 o = {pk2(v[0], v[1]), pk2(v[2], v[3]), pk2(v[4], v[5]), pk2(v[6], v[7])};
                    int k32 = (f0 + 96 * wn) / 32 + P;
                    u32 so = (u32)(k32 >> 1) * 16384u + (u32)((4 * (wm & 1) + i) * 2 + (k32 & 1)) * 1024u;
                    __builtin_amdgcn_raw_buffer_store_b128(o, rs_ws, hb + so, 0, 16);
                }
            }
        }
        WAIT_V(0); BAR();
        if (tid == 0) st_flag(ws + WS_FH + ((xcd * 4 + rb) * 8 + cb) * 4, tag);
        STAMP(4); STAMP(5);
    } else {
        const int mat = kap >> 1;
        row0 = 1024 * mu + 512 * (kap & 1) + 256 * (g >> 2);
        const rsrc_t rs_d = mk_rsrc(mat ? Di : Dr, D_ * D_ * 2);
        const int rperm = 32 * (w >> 1) + 4 * (w & 1) + 8 * (l >> 5) + ((l >> 3) & 3);
        u32 vA0 = (u32)rperm * 1152u + 16u * ((l & 7) ^ q);
        u32 vA1 = (u32)(rperm + 16) * 1152u + 16u * ((l & 7) ^ (4 + q));
        const u32 sA = (u32)row0 * 1152u;
        u32 vB[2], vBs[2];
#pragma unroll
        for (int hf = 0; hf < 2; ++hf) {
            int p = 9 * hf + w, rho = 8 * p + (l >> 3);
            vB[hf] = (u32)(144 * nb + rho) * 1152u + 16u * ((l & 7) ^ ((rho >> 1) & 7));
            int rho2 = 8 * (9 * hf + 8) + w;
            vBs[hf] = (u32)(144 * nb + rho2) * 1152u + 16u * ((l & 7) ^ ((rho2 >> 1) & 7));
        }
        auto issueA = [&](int unit, int hf) {
            u32 ld = (unit % 3) * 32768u + (16 * hf + 2 * w) * 1024u, so = sA + hf * (128u * 1152u) + unit * 128u;
            dma<0>(rs_x, ld, vA0, so); dma<0>(rs_x, ld + 1024, vA1, so);
        };
        auto issueB = [&](int unit, int hf) {
            u32 lb = L_B1 + (unit % 3) * 18432u + 9216u * hf, so = unit * 128u;
            dma<0>(rs_d, lb + w * 1024, hf ? vB[1] : vB[0], so);
            if (l < 8) dma<0>(rs_d, lb + 8192 + w * 128, hf ? vBs[1] : vBs[0], so);
        };
#pragma unroll
        for (int un = 0; un < 3; ++un) { issueA(un, 0); issueA(un, 1); issueB(un, 0); issueB(un, 1); }

        asm volatile("s_waitcnt vmcnt(24)" : "+v"(epoch), "+v"(rowv), "+v"(rowt) :: "memory");
        tag = __builtin_amdgcn_readfirstlane(epoch) + 1u;
        {
            float v[9];
#pragma unroll
            for (int i = 0; i < 4; ++i) { v[2 * i] = bflo(rowv[i]); v[2 * i + 1] = bfhi(rowv[i]); }
            v[8] = bflo(rowt);
            float s = 0.f;
#pragma unroll
            for (int i = 0; i < 9; ++i) s += v[i];
            float mean = wave_sum(s) * (1.f / D_), ss = 0.f;
#pragma unroll
            for (int i = 0; i < 9; ++i) { float d = v[i] - mean; ss += d * d; }
            float rstd = 1.0f / sqrtf(wave_sum(ss) * (1.f / D_) + 1e-5f);
            u32x4 gr = {__float_as_uint(mean), tag, __float_as_uint(rstd), tag};
            if (l == 0) __builtin_amdgcn_raw_buffer_store_b128(gr, rs_ws, WS_STAT + (bid * 8 + w) * 16, 0, 16);
        }

        const u32 sw = 16u * (q ^ (r16 >> 1));
        const u32 rA01 = (u32)(64 * wm + 32 * wn + r16) * 128u + sw, rA23 = (u32)(64 * wm + 32 * (1 - wn) + r16) * 128u + sw;
        const u32 rB0 = L_B1 + (u32)(64 * wn + r16) * 128u + sw, rB8 = L_B1 + (u32)(128 + r16) * 128u + sw;
        f32x4 acc[4][4], acc8[2];
#pragma unroll
        for (int i = 0; i < 4; ++i)
#pragma unroll
            for (int t = 0; t < 4; ++t) acc[i][t] = (f32x4){0.f, 0.f, 0.f, 0.f};
        acc8[0] = acc8[1] = (f32x4){0.f, 0.f, 0.f, 0.f};
        float csA = 0.f, csB = 0.f;
        bf16x8 fa[2][4], fb[2][5];
        auto step = [&](const int set, int u) {
            if (u < 15) WAIT_V(8); else WAIT_V(4);
            WAIT_L0(); BAR(); SB();
            const int un = (u < 17) ? u + 1 : u;
            const u32 sl = (un >> 1) % 3, xs = (un & 1) * 64u;
            const u32 a01 = sl * 32768u + (rA01 ^ xs), a23 = sl * 32768u + (rA23 ^ xs), b0a = sl * 18432u + (rB0 ^ xs), b8a = sl * 18432u + (rB8 ^ xs);
            acc[0][0] = mfma(fa[set][0], fb[set][0], acc[0][0]); acc[1][0] = mfma(fa[set][1], fb[set][0], acc[1][0]); SB();
            if (u >= 1 && u <= 12) { issueA(((u - 1) >> 1) + 3, set ^ 1); issueB(((u - 1) >> 1) + 3, set ^ 1); }
            else if (u >= 13 && u <= 16) {
                int j = u - 13; u32 ld = j * 16384u + 2 * w * 1024u, so = sC + 128u * j;
                dma<0>(rs_c, ld, vC0, so); dma<0>(rs_c, ld + 1024, vC1, so);
            }
            SB();
#pragma unroll
            for (int m = 2; m < 18; ++m) {
                const int t = m >> 2, i = m & 3, rr = m - 2;
                if (t < 4) acc[i][t & 3] = mfma(fa[set][i], fb[set][t], acc[i][t & 3]); else acc8[i & 1] = mfma(fa[set][i], fb[set][4], acc8[i & 1]);
                if (rr < 2) DSR128(fa[set ^ 1][rr], a01, 2048 * rr);
                else if (rr < 4) DSR128(fa[set ^ 1][rr], a23, 2048 * (rr - 2));
                else if (rr < 8) DSR128(fb[set ^ 1][rr - 4], b0a, 2048 * (rr - 4));
                else if (rr == 8) DSR128(fb[set ^ 1][4], b8a, 0);
                SB();
            }
            const u32x4 b0 = __builtin_bit_cast(u32x4, fb[set][0]), b1 = __builtin_bit_cast(u32x4, fb[set][1]), b2 = __builtin_bit_cast(u32x4, fb[set][2]);
            const u32x4 b3 = __builtin_bit_cast(u32x4, fb[set][3]), b4 = __builtin_bit_cast(u32x4, fb[set][4]);
            if (wm == 0) { DOT4(csA, b0); DOT1(csB, b4[0]); }
            else if (wm == 1) { DOT4(csA, b1); DOT1(csB, b4[1]); }
            else if (wm == 2) { DOT4(csA, b2); DOT1(csB, b4[2]); }
            else { DOT4(csA, b3); DOT1(csB, b4[3]); }
        };
        WAIT_V(17); BAR();
        STAMP(1);
        DSR128(fa[0][0], rA01, 0); DSR128(fa[0][1], rA01, 2048); DSR128(fa[0][2], rA23, 0); DSR128(fa[0][3], rA23, 2048);
#pragma unroll
        for (int t = 0; t < 4; ++t) DSR128(fb[0][t], rB0, 2048 * t);
        DSR128(fb[0][4], rB8, 0);
#pragma unroll 1
        for (int u = 0; u < 18; u += 2) { step(0, u); step(1, u + 1); }
        asm volatile("s_waitcnt lgkmcnt(0)" :: "v"(fa[0][0]), "v"(fa[0][1]), "v"(fa[0][2]), "v"(fa[0][3]), "v"(fb[0][0]), "v"(fb[0][1]), "v"(fb[0][2]), "v"(fb[0][3]), "v"(fb[0][4]) : "memory");
        BAR();

        STAMP(2);
        const int qd = 2 * mu + (g >> 2);
        {
            const rsrc_t rs_w2 = mk_rsrc(w2, F_ * D_ * 2);
            const u32 so = (u32)(1152 * kap + 288 * qd) * 1152u + 288u * nb;
#pragma unroll 1
            for (int p = w; p < 81; p += 8) {
                u32 o = p * 1024 + l * 16, row = o / 288u, ch = o - row * 288u;
                dma<0>(rs_w2, L_B2 + p * 1024, row * 1152u + ch, so);
            }
        }
        {
            float a = csA, b = csB;
            a += __shfl_xor(a, 16); a += __shfl_xor(a, 32); b += __shfl_xor(b, 16); b += __shfl_xor(b, 32);
            if (l < 16) {
                *(LAS float*)((LAS char*)smem + L_CS + (64 * wn + 16 * wm + l) * 4) = a;
                if (wn == 0) *(LAS float*)((LAS char*)smem + L_CS + (128 + 16 * wm + l) * 4) = b;
            }
        }
        if (w < 4) {
            u32x4 gr;
            for (int it = 0; it < SPIN_MAX; ++it) {
                gr = __builtin_amdgcn_raw_buffer_load_b128(rs_ws, WS_STAT + (row0 + tid) * 16, 0, 16);
                if (__all(gr[1] == tag && gr[3] == tag)) break;
                __builtin_amdgcn_s_sleep(2);
            }
            *(LAS u32x2*)((LAS char*)smem + L_STAT + tid * 8) = (u32x2){gr[0], gr[2]};
        }
        WAIT_L0(); BAR();
        STAMP(3);
        const u32 bmb = WS_BM + (u32)((kap * 4 + nb) * 68) * 9216u + l * 16;
        {
            float cs[5];
#pragma unroll
            for (int t = 0; t < 4; ++t) cs[t] = *(LAS float*)((LAS char*)smem + L_CS + (64 * wn + 16 * t + r16) * 4);
            {
                LAS float* c8 = (LAS float*)((LAS char*)smem + L_CS + (128 + r16) * 4);
                cs[4] = (c8[0] + c8[16]) + (c8[32] + c8[48]);
            }
            const float sgn = mat ? -1.f : 1.f;
#pragma unroll
            for (int Pi = 0; Pi < 2; ++Pi) {
                int sb = 64 * wm + 32 * ((Pi + wn) & 1);
                float mean[8], rs[8];
#pragma unroll
                for (int e = 0; e < 4; ++e) {
                    f32x4 st = *(LAS f32x4*)((LAS char*)smem + L_STAT + (sb + 8 * q + 2 * e) * 8);
                    mean[2 * e] = st[0]; rs[2 * e] = st[1] * sgn; mean[2 * e + 1] = st[2]; rs[2 * e + 1] = st[3] * sgn;
                }
                int kb = 16 * mu + 8 * (g >> 2) + (sb >> 5);
#pragma unroll
                for (int t = 0; t < 5; ++t) {
                    if (t == 4 && Pi == 1) break;
                    f32x4 lo = (t < 4) ? acc[2 * Pi][t & 3] : acc8[0], hi = (t < 4) ? acc[2 * Pi + 1][t & 3] : acc8[1];
                    float v[8];
#pragma unroll
                    for (int e = 0; e < 4; ++e) { v[e] = (lo[e] - mean[e] * cs[t]) * rs[e]; v[4 + e] = (hi[e] - mean[4 + e] * cs[t]) * rs[4 + e]; }
                    u32x4 o = {pk2(v[0], v[1]), pk2(v[2], v[3]), pk2(v[4], v[5]), pk2(v[6], v[7])};
                    int nf = (t < 4) ? 4 * wn + t : 8;
                    __builtin_amdgcn_raw_buffer_store_b128(o, rs_ws, bmb + (u32)(kb * 9 + nf) * 1024u, 0, 16);
                }
            }
        }
        WAIT_V(0); BAR();
        if (tid == 0) st_flag(ws + WS_FAB + ((kap * 4 + nb) * 4 + qd) * 4, tag);
        STAMP(4);
        {
            const u32 ra = L_B2 + (u32)(4 * q + ((l >> 2) & 3)) * 288u + 8u * (l & 3);
#pragma unroll 1
            for (int b = w; b < 81; b += 8) {
                int kb = b / 9, nf = b - kb * 9;
                u32 a0 = ra + kb * 9216 + nf * 32;
                bf16x8 v = ldstr(a0, a0 + 4608);
                __builtin_amdgcn_raw_buffer_store_b128(__builtin_bit_cast(u32x4, v), rs_ws, bmb + (u32)((32 + 9 * qd) * 9 + b) * 1024u, 0, 16);
            }
        }
        WAIT_V(0); WAIT_L0(); BAR();
        if (tid == 0) st_flag(ws + WS_FW + ((kap * 4 + nb) * 4 + qd) * 4, tag);
        STAMP(5);
    }

    if (bid == 255 && w == 7) {
#pragma unroll 1
        for (int c = 0; c < 4; ++c) poll(ws + WS_STAT + (size_t)(c * 64 + l) * 128 + 4, true, tag);
        if (l == 0) st_flag(ws + WS_EPOCH, tag);
    }

    {
        const int gam = w >> 2, mh = (w >> 1) & 1, nh = w & 1;
        const u32 sB = WS_BM + (u32)((kap * 4 + nb) * 68) * 9216u;
        const u32 vB = (u32)(2 * w) * 1024u + l * 16, vBq = (u32)(16 + (w >> 2)) * 1024u + (w & 3) * 256u + l * 16;
        auto issueB = [&](int j) {
            u32 lb = L_B2 + (j & 3) * 18432u, so = sB + (u32)j * 18432u;
            dma<16>(rs_ws, lb + 2 * w * 1024, vB, so); dma<16>(rs_ws, lb + 2 * w * 1024 + 1024, vB + 1024, so);
            if (l < 16) dma<16>(rs_ws, lb + (16 + (w >> 2)) * 1024 + (w & 3) * 256, vBq, so);
        };
        const u32 sH = WS_H + (u32)((t2 >> 7) * 72 + 18 * kap) * 16384u;
        auto issueA = [&](int j) {
            u32 ld = (j & 3) * 16384u + 2 * w * 1024u;
            if (j < 16) { u32 so = sC + 2048u * (j >> 3) + 128u * (j & 7); dma<0>(rs_c, ld, vC0, so); dma<0>(rs_c, ld + 1024, vC1, so); }
            else { u32 so = sH + (u32)(j - 16) * 16384u; dma<16>(rs_ws, ld, vB, so); dma<16>(rs_ws, ld + 1024, vB + 1024, so); }
        };
        poll(ws + WS_FAB + ((kap * 4 + nb) * 4 + (l & 3)) * 4, l < 4, tag);
        STAMP(6);
        issueB(0); issueB(1); issueB(2); issueB(3);

        const u32 sw = 16u * ((4 * gam + q) ^ (r16 >> 1));
        u32 rA01 = (u32)(64 * mh + 32 * nh + r16) * 128u + sw, rA23 = (u32)(64 * mh + 32 * (1 - nh) + r16) * 128u + sw;
        const u32 rB0 = L_B2 + (u32)(gam * 9 + 4 * nh) * 1024u + l * 16, rB8 = L_B2 + (u32)(gam * 9 + 8) * 1024u + l * 16;
        f32x4 acc[4][4], acc8[2];
#pragma unroll
        for (int i = 0; i < 4; ++i)
#pragma unroll
            for (int t = 0; t < 4; ++t) acc[i][t] = (f32x4){0.f, 0.f, 0.f, 0.f};
        acc8[0] = acc8[1] = (f32x4){0.f, 0.f, 0.f, 0.f};
        bf16x8 fa[2][4], fb[2][5];
        auto step = [&](const int set, int i) {
            if (i <= 30) WAIT_V(10); else WAIT_V(0);
            WAIT_L0(); BAR(); SB();
            if (i == 15) {
                rA01 = (u32)((4 * mh + 2 * nh) * 2 + gam) * 1024u + l * 16; rA23 = (u32)((4 * mh + 2 * (1 - nh)) * 2 + gam) * 1024u + l * 16;
            }
            const u32 sl = ((i < 33) ? i + 1 : i) & 3;
            const u32 a01 = sl * 16384u + rA01, a23 = sl * 16384u + rA23, b0a = sl * 18432u + rB0, b8a = sl * 18432u + rB8;
            acc[0][0] = mfma(fb[set][0], fa[set][0], acc[0][0]); acc[1][0] = mfma(fb[set][0], fa[set][1], acc[1][0]); SB();
            if (i == 12) {
                const char* fp = (l < 8) ? ws + WS_FH + ((xcd * 4 + (mb >> 1)) * 8 + l) * 4 : ws + WS_FW + ((kap * 4 + nb) * 4 + (l & 3)) * 4;
                STAMP(8);
                poll(fp, l < 6 || (l >= 8 && l < 12), tag);
                STAMP(9);
            }
            if (i < 30) issueA(i + 4);
            SB();
            acc[2][0] = mfma(fb[set][0], fa[set][2], acc[2][0]); acc[3][0] = mfma(fb[set][0], fa[set][3], acc[3][0]); SB();
            if (i < 30) issueB(i + 4);
            SB();
#pragma unroll
            for (int m = 4; m < 18; ++m) {
                const int t = m >> 2, ii = m & 3, rr = m - 4;
                if (t < 4) acc[ii][t & 3] = mfma(fb[set][t], fa[set][ii], acc[ii][t & 3]); else acc8[ii & 1] = mfma(fb[set][4], fa[set][ii], acc8[ii & 1]);
                if (rr < 2) DSR128(fa[set ^ 1][rr], a01, 2048 * rr);
                else if (rr < 4) DSR128(fa[set ^ 1][rr], a23, 2048 * (rr - 2));
                else if (rr < 8) DSR128(fb[set ^ 1][rr - 4], b0a, 1024 * (rr - 4));
                else if (rr == 8) DSR128(fb[set ^ 1][4], b8a, 0);
                SB();
            }
        };
        WAIT_V(9); BAR();
        STAMP(7);
        DSR128(fa[0][0], rA01, 0); DSR128(fa[0][1], rA01, 2048); DSR128(fa[0][2], rA23, 0); DSR128(fa[0][3], rA23, 2048);
#pragma unroll
        for (int t = 0; t < 4; ++t) DSR128(fb[0][t], rB0, 1024 * t);
        DSR128(fb[0][4], rB8, 0);
#pragma unroll 1
        for (int i = 0; i < 34; i += 2) { step(0, i); step(1, i + 1); }
        asm volatile("s_waitcnt lgkmcnt(0)" :: "v"(fa[0][0]), "v"(fa[0][1]), "v"(fa[0][2]), "v"(fa[0][3]), "v"(fb[0][0]), "v"(fb[0][1]), "v"(fb[0][2]), "v"(fb[0][3]), "v"(fb[0][4]) : "memory");

        STAMP(10);
        const int wp = w & 3;
        const int tile = (mu * 8 + mb) * 4 + nb;
        auto blk = [&](int b) -> u32x4 {
            f32x4 lo = (b < 8) ? acc[2 * (b >> 2)][b & 3] : acc8[0], hi = (b < 8) ? acc[2 * (b >> 2) + 1][b & 3] : acc8[1];
            return (u32x4){pk2(lo[0], lo[1]), pk2(lo[2], lo[3]), pk2(hi[0], hi[1]), pk2(hi[2], hi[3])};
        };
        BAR();
        LAS u32x4* xb = (LAS u32x4*)((LAS char*)smem + (u32)(w * 9) * 1024u + l * 16);
        const bool own = (wp == kap);
#pragma unroll
        for (int b = 0; b < 9; ++b) {
            bool mine = (gam == 0) ? (b < 5) : (b >= 5);
            if (own || !mine) xb[b * 64] = blk(b);
        }
        WAIT_L0(); BAR();
        if (!own) {
            LAS u32x4* pb = (LAS u32x4*)((LAS char*)smem + (u32)((w ^ 4) * 9) * 1024u + l * 16);
            const u32 pso = WS_P + (u32)((tile * 36 + wp * 9) * 4 + kap) * 1024u;
#pragma unroll
            for (int b = 0; b < 9; ++b) {
                bool mine = (gam == 0) ? (b < 5) : (b >= 5);
                if (mine) {
                    u32x4 o = pb[b * 64];
                    f32x4 lo = (b < 8) ? acc[2 * (b >> 2)][b & 3] : acc8[0], hi = (b < 8) ? acc[2 * (b >> 2) + 1][b & 3] : acc8[1];
                    u32x4 s = {pk2(lo[0] + bflo(o[0]), lo[1] + bfhi(o[0])), pk2(lo[2] + bflo(o[1]), lo[3] + bfhi(o[1])),
                               pk2(hi[0] + bflo(o[2]), hi[1] + bfhi(o[2])), pk2(hi[2] + bflo(o[3]), hi[3] + bfhi(o[3]))};
                    __builtin_amdgcn_raw_buffer_store_b128(s, rs_ws, l * 16 + pso + b * 4096u, 0, 16);
                }
            }
        }
        WAIT_V(0); BAR();
        STAMP(11);
        if (tid == 0) st_flag(ws + WS_FP + (tile * 4 + kap) * 4, tag);
        poll(ws + WS_FP + (tile * 4 + (l & 3)) * 4, l < 4, tag);
        STAMP(12);
        const int omh = kap >> 1, onh = kap & 1;
#pragma unroll 1
        for (int b = w; b < 9; b += 8) {
            u32x4 p[3];
#pragma unroll
            for (int k = 0; k < 3; ++k) {
                int kk = (kap + 1 + k) & 3;
                p[k] = __builtin_amdgcn_raw_buffer_load_b128(rs_ws, l * 16, WS_P + (u32)((tile * 36 + kap * 9 + b) * 4 + kk) * 1024u, 16);
            }
            int P = (b < 8) ? (b >> 2) : 0, nf = (b < 8) ? 4 * onh + (b & 3) : 8;
            int trow = t2 + 64 * omh + 32 * ((P + onh) & 1) + r16, col = 144 * nb + 16 * nf + 4 * q;
            u32x2 x0 = *(const u32x2*)(x + (size_t)trow * D_ + col), x1 = *(const u32x2*)(x + (size_t)(trow + 16) * D_ + col);
            u32x4 o0 = *(LAS u32x4*)((LAS char*)smem + (u32)(kap * 9 + b) * 1024u + l * 16);
            u32x4 o1 = *(LAS u32x4*)((LAS char*)smem + (u32)((kap + 4) * 9 + b) * 1024u + l * 16);
            u32x4 xx = {x0[0], x0[1], x1[0], x1[1]};
            u32 rr[4];
#pragma unroll
            for (int d = 0; d < 4; ++d) {
                float lo = ((bflo(o0[d]) + bflo(o1[d])) + (bflo(p[0][d]) + bflo(p[1][d]))) + (bflo(p[2][d]) + bflo(xx[d]));
                float hi = ((bfhi(o0[d]) + bfhi(o1[d])) + (bfhi(p[0][d]) + bfhi(p[1][d]))) + (bfhi(p[2][d]) + bfhi(xx[d]));
                rr[d] = pk2(lo, hi);
            }
            { const rsrc_t rs_o = mk_rsrc(out, S_ * D_ * 2);
            __builtin_amdgcn_raw_buffer_store_b64((u32x2){rr[0], rr[1]}, rs_o, (trow * D_ + col) * 2, 0, OAUX);
            __builtin_amdgcn_raw_buffer_store_b64((u32x2){rr[2], rr[3]}, rs_o, ((trow + 16) * D_ + col) * 2, 0, OAUX); }
        }
        WAIT_V(0); STAMP(13);
    }
}

extern "C" void kernel_launch(void* const* d_in, const int* in_sizes, int n_in, void* d_out, int out_size, void* d_ws, size_t ws_size,
                              hipStream_t stream) {
    (void)hipFuncSetAttribute((const void*)fused_kernel, hipFuncAttributeMaxDynamicSharedMemorySize, LDS_BYTES);
    hipLaunchKernelGGL(fused_kernel, dim3(256), dim3(512), LDS_BYTES, stream, (const u16*)d_in[0], (const u16*)d_in[1], (const u16*)d_in[2],
                       (const u16*)d_in[3], (const u16*)d_in[4], (const u16*)d_in[6], (const u16*)d_in[7], (u16*)d_out, (char*)d_ws);
}
```

```cpp
#include <hip/hip_runtime.h>
typedef unsigned short u16;
typedef unsigned u32;
typedef short bf16x8 __attribute__((ext_vector_type(8)));
typedef short s16x4 __attribute__((ext_vector_type(4)));
typedef short s16x2 __attribute__((ext_vector_type(2)));
typedef __bf16 bf16x2 __attribute__((ext_vector_type(2)));
typedef float f32x4 __attribute__((ext_vector_type(4)));
typedef float f32x2 __attribute__((ext_vector_type(2)));
typedef unsigned u32x4 __attribute__((ext_vector_type(4)));
typedef unsigned u32x2 __attribute__((ext_vector_type(2)));
#define LAS __attribute__((address_space(3)))
#define DEVI __device__ __forceinline__

constexpr int S_ = 2048, D_ = 576, F_ = 4608;
constexpr u32 WS_EPOCH = 0;
constexpr u32 WS_FAB = 4096;
constexpr u32 WS_FH = 8192;
constexpr u32 WS_FP = 16384;
constexpr u32 WS_STAT = 32768;
constexpr u32 WS_DS = 65536;
constexpr u32 WS_CP = 131072;
constexpr u32 WS_AB = 1u << 20;
constexpr u32 WS_H = 8u << 20;
constexpr u32 WS_P = 32u << 20;
constexpr u32 WS_END = 52u << 20;
constexpr u32 L_B = 98304;
constexpr u32 L_STAT = 159744;
constexpr u32 L_CS = 161792;
constexpr u32 L_TAG = 163328;
constexpr u32 LDS_BYTES = 163840;
constexpr int SPIN_MAX = 1 << 20;

extern __shared__ __attribute__((aligned(16))) char smem[];

#define WAIT_V(n) asm volatile("s_waitcnt vmcnt(" #n ")" ::: "memory")
#define WAIT_L(n) asm volatile("s_waitcnt lgkmcnt(" #n ")" ::: "memory")
#define BAR() __builtin_amdgcn_s_barrier()
#define STAMP(i)

#define TRC()
#define ABAR() do { asm volatile("" ::: "memory"); SB(); TRC(); __builtin_amdgcn_s_barrier(); TRC(); SB(); asm volatile("" ::: "memory"); } while (0)
#define SB() __builtin_amdgcn_sched_barrier(0)
#define DSR128(dst, addr, off) asm volatile("ds_read_b128 %0, %1 offset:%2" : "=v"(dst) : "v"(addr), "i"(off) : "memory")
#define DSRTR(dst, addr, off) asm volatile("ds_read_b64_tr_b16 %0, %1 offset:%2" : "=v"(dst) : "v"(addr), "i"(off) : "memory")
#define MFMA(c, a, b) asm volatile("v_mfma_f32_16x16x32_bf16 %0, %1, %2, %0" : "+v"(c) : "v"(a), "v"(b))
#define PIN(v) asm volatile("" : "+v"(v))
#define CAT(lo, hi) __builtin_shufflevector(lo, hi, 0, 1, 2, 3, 4, 5, 6, 7)
#define UNI(v) ((u32)__builtin_amdgcn_readfirstlane(v))
typedef __amdgpu_buffer_rsrc_t rsrc_t;
DEVI rsrc_t mk_rsrc(const void* p, u32 bytes) { return __builtin_amdgcn_make_buffer_rsrc((void*)p, 0, bytes, 0x00020000); }
template <int AUX> DEVI void dma(rsrc_t rs, u32 lds, u32 voff, u32 soff) {
    __builtin_amdgcn_raw_ptr_buffer_load_lds(rs, (LAS void*)((LAS char*)smem + lds), 16, voff, soff, 0, AUX);
}
DEVI f32x4 mfma(bf16x8 a, bf16x8 b, f32x4 c) { return __builtin_amdgcn_mfma_f32_16x16x32_bf16(a, b, c, 0, 0, 0); }
DEVI u32 pk2(float a, float b) { bf16x2 v = {(__bf16)a, (__bf16)b}; return __builtin_bit_cast(u32, v); }
DEVI float bflo(u32 v) { return __uint_as_float(v << 16); }
DEVI float bfhi(u32 v) { return __uint_as_float(v & 0xffff0000u); }
DEVI float dot1(u32 v, float acc) {
    return __builtin_amdgcn_fdot2_f32_bf16(__builtin_bit_cast(bf16x2, v), __builtin_bit_cast(bf16x2, 0x3f803f80u), acc, false);
}
DEVI f32x2 add2(u32 v, f32x2 acc) { return acc + (f32x2){bflo(v), bfhi(v)}; }
template <int CTRL> DEVI float dpp_add(float v) {
    return v + __builtin_bit_cast(float, __builtin_amdgcn_update_dpp(0, __builtin_bit_cast(int, v), CTRL, 0xf, 0xf, true));
}
DEVI float row16_sum(float v) { v = dpp_add<0xB1>(v); v = dpp_add<0x4E>(v); v = dpp_add<0x141>(v); return dpp_add<0x140>(v); }
DEVI u32 ld_flag(const char* p) { return __hip_atomic_load((const u32*)p, __ATOMIC_RELAXED, __HIP_MEMORY_SCOPE_AGENT); }
DEVI void st_flag(char* p, u32 v) { __hip_atomic_store((u32*)p, v, __ATOMIC_RELAXED, __HIP_MEMORY_SCOPE_AGENT); }
DEVI void poll(const char* p, u32 tag) {
#pragma unroll 1
    for (int it = 0; it < SPIN_MAX; ++it) {
        u32 f = ld_flag(p);
        if (__all(f == tag)) break;
        __builtin_amdgcn_s_sleep(1);
    }
}
template <typename T> DEVI T lds_ld(u32 a) { return *(LAS T*)((LAS char*)smem + a); }
template <typename T> DEVI void lds_st(u32 a, T v) { *(LAS T*)((LAS char*)smem + a) = v; }
DEVI u32 colmap(int n, int nf) {
    const u32 pc = (n == 1 || n == 2) ? 0x3021u : 0x3210u, o = n & 1;
    return nf < 8 ? 16u * o + 32u * ((pc >> (4 * (nf >> 1))) & 3u) + 16u * (nf & 1) : (o ? 0u : 128u);
}

__global__ __launch_bounds__(768) void fused_kernel(const u16* __restrict__ x, const u16* __restrict__ Cr, const u16* __restrict__ Ci,
                                                    const u16* __restrict__ Dr, const u16* __restrict__ Di, const u16* __restrict__ w1,
                                                    const u16* __restrict__ w2, u16* __restrict__ out, char* __restrict__ ws) {
    const int tid = threadIdx.x, l = tid & 63, w = __builtin_amdgcn_readfirstlane(tid >> 6);
    const int bid = blockIdx.x, j = bid & 7, g = bid >> 3, mb = g >> 2, nb = g & 3;
    const int q = l >> 4, r16 = l & 15;
    const bool is_ab = nb == 3;
    const int row0 = is_ab ? 512 * (j & 3) + 256 * (mb >> 2) : 256 * mb;
    const int c0 = is_ab ? 0 : 3;
    const rsrc_t rs_ws = mk_rsrc(ws, WS_END);
    const u32 l16 = l * 16;
    STAMP(0);
    u32 tag;

    if (w < 8) {
        const int wm = w & 3, wn = w >> 2;
        const u32 trl = (u32)((l >> 2) & 7) * 64u + 32u * (q & 1) + 8u * (l & 3);
        const u32 rowl = (u32)r16 * 128u + 16u * (q ^ (r16 >> 1));
        if (!is_ab) {
            const u32 rA = (u32)wm * 8192u + rowl;
            const u32 rB = L_B + (u32)(l >> 5) * 6144u + (u32)wn * 1536u + trl;
            f32x4 acc[4][6];
#pragma unroll
            for (int i = 0; i < 4; ++i)
#pragma unroll
                for (int t = 0; t < 6; ++t) { acc[i][t] = (f32x4){0.f, 0.f, 0.f, 0.f}; PIN(acc[i][t]); }
            bf16x8 fa[2][4]; s16x4 fl[6], fh[6];
            auto unit = [&](const int set, u32 a, u32 b0, u32 b1) {
#pragma unroll
                for (int t = 0; t < 6; ++t) {
                    if (t < 2) WAIT_L(10); else WAIT_L(14);
                    SB();
                    const bf16x8 fb = CAT(fl[t], fh[t]);
#pragma unroll
                    for (int i = 0; i < 4; ++i) {
                        acc[i][t] = mfma(fb, fa[set][i], acc[i][t]);
                        if (t == 1) DSR128(fa[set ^ 1][i], a, 2048 * i);
                        SB();
                    }
                    if (t == 0 && set == 1) { WAIT_L(0); BAR(); SB(); }
                    DSRTR(fl[t], (t & 1) ? b1 : b0, 512 * (t >> 1));
                    DSRTR(fh[t], (t & 1) ? b1 : b0, 512 * (t >> 1) + 3072);
                    SB();
                }
            };
            BAR();
            STAMP(1);
            WAIT_L(0);
            DSRTR(fl[0], rB, 0); DSRTR(fh[0], rB, 3072);
#pragma unroll
            for (int i = 0; i < 4; ++i) DSR128(fa[0][i], rA, 2048 * i);
#pragma unroll
            for (int t = 1; t < 6; ++t) { DSRTR(fl[t], rB ^ (32u * (t & 1)), 512 * (t >> 1)); DSRTR(fh[t], rB ^ (32u * (t & 1)), 512 * (t >> 1) + 3072); }
            u32 as = 0, bs = 12288;
#pragma unroll 1
            for (int s = 0; s < 9; ++s) {
                unit(0, (rA ^ 64u) + as, rB + bs, (rB ^ 32u) + bs);
                as = (as == 65536u) ? 0u : as + 32768u;
                bs = (bs == 49152u) ? 0u : bs + 12288u;
                unit(1, rA + as, rB + bs, (rB ^ 32u) + bs);
                bs = (bs == 49152u) ? 0u : bs + 12288u;
            }
            asm volatile("s_waitcnt lgkmcnt(0)" :: "v"(fa[0][0]), "v"(fa[0][1]), "v"(fa[0][2]), "v"(fa[0][3]), "v"(fl[0]), "v"(fl[1]), "v"(fl[2]), "v"(fl[3]),
                         "v"(fl[4]), "v"(fl[5]), "v"(fh[0]), "v"(fh[1]), "v"(fh[2]), "v"(fh[3]), "v"(fh[4]), "v"(fh[5]) : "memory");
            STAMP(2);
            {
                f32x4 cs[6];
#pragma unroll
                for (int t = 0; t < 6; ++t) cs[t] = lds_ld<f32x4>(L_CS + (96 * wn + 16 * t + 4 * q) * 4) + lds_ld<f32x4>(L_CS + 768 + (96 * wn + 16 * t + 4 * q) * 4);
#pragma unroll
                for (int i = 0; i < 4; ++i) {
                    const f32x2 st = lds_ld<f32x2>(L_STAT + (64 * wm + 16 * i + r16) * 8);
                    const f32x2 nm = {-st[0], -st[0]}, rs = {st[1], st[1]};
#pragma unroll
                    for (int P = 0; P < 3; ++P) {
                        u32x4 o;
#pragma unroll
                        for (int e = 0; e < 4; ++e) {
                            const f32x4 av = acc[i][2 * P + (e >> 1)], cv = cs[2 * P + (e >> 1)];
                            f32x2 a2 = {av[2 * (e & 1)], av[2 * (e & 1) + 1]}, c2 = {cv[2 * (e & 1)], cv[2 * (e & 1) + 1]};
                            f32x2 v = __builtin_elementwise_fma(c2, nm, a2) * rs;
                            s16x2 pk = __builtin_bit_cast(s16x2, __builtin_convertvector(v, bf16x2));
                            pk = __builtin_elementwise_max(pk, (s16x2){0, 0});
                            o[e] = __builtin_bit_cast(u32, pk);
                        }
                        const u32 k32 = 3 * wn + P;
                        lds_st<u32x4>((k32 >> 1) * 32768u + (k32 & 1) * 1024u + (u32)(4 * wm + i) * 2048u + l16, o);
                    }
                }
            }
            tag = __builtin_amdgcn_readfirstlane(lds_ld<u32>(L_TAG));
        } else {
            const u32 rA = (u32)wm * 8192u + (u32)wn * 4096u + rowl;
            const u32 rB = L_B + (u32)wn * 8192u + rowl, rS = L_B + 16384u + rowl;
            f32x4 acc[4][4], acc8[2];
#pragma unroll
            for (int i = 0; i < 4; ++i)
#pragma unroll
                for (int t = 0; t < 4; ++t) { acc[i][t] = (f32x4){0.f, 0.f, 0.f, 0.f}; PIN(acc[i][t]); }
            acc8[0] = acc8[1] = (f32x4){0.f, 0.f, 0.f, 0.f}; PIN(acc8[0]); PIN(acc8[1]);
            bf16x8 fa[2][4], fb[2][5];
            auto half = [&](const int set, u32 a01, u32 a23, u32 b0, u32 b8) {
#pragma unroll
                for (int m = 0; m < 18; ++m) {
                    const int t = m >> 2, i = m & 3;
                    if (t < 4) acc[i][t & 3] = mfma(fa[set][i], fb[set][t], acc[i][t & 3]); else acc8[i & 1] = mfma(fa[set][i], fb[set][4], acc8[i & 1]);
                    if (m < 2) DSR128(fa[set ^ 1][m], a01, 2048 * m);
                    else if (m < 4) DSR128(fa[set ^ 1][m], a23, 2048 * (m - 2));
                    else if (m < 8) DSR128(fb[set ^ 1][m - 4], b0, 2048 * (m - 4));
                    else if (m == 8) DSR128(fb[set ^ 1][4], b8, 0);
                    SB();
                }
            };
            BAR();
            STAMP(1);
            u32 as = 0, bs = 0;
            DSR128(fa[0][0], rA, 0); DSR128(fa[0][1], rA, 2048); DSR128(fa[0][2], rA ^ 4096u, 0); DSR128(fa[0][3], rA ^ 4096u, 2048);
#pragma unroll
            for (int t = 0; t < 4; ++t) DSR128(fb[0][t], rB, 2048 * t);
            DSR128(fb[0][4], rS, 0);
#pragma unroll 1
            for (int s = 0; s < 9; ++s) {
                WAIT_L(0); SB();
                half(0, (rA ^ 64u) + as, (rA ^ 4160u) + as, (rB ^ 64u) + bs, (rS ^ 64u) + bs);
                WAIT_L(0); BAR(); SB();
                as = (as == 65536u) ? 0u : as + 32768u;
                bs = (bs == 36864u) ? 0u : bs + 18432u;
                half(1, rA + as, (rA ^ 4096u) + as, rB + bs, rS + bs);
            }
            asm volatile("s_waitcnt lgkmcnt(0)" :: "v"(fa[0][0]), "v"(fa[0][1]), "v"(fa[0][2]), "v"(fa[0][3]), "v"(fb[0][0]), "v"(fb[0][1]), "v"(fb[0][2]), "v"(fb[0][3]), "v"(fb[0][4]) : "memory");
            STAMP(2);
            {
                float cs[5];
#pragma unroll
                for (int t = 0; t < 5; ++t) {
                    const u32 ca = L_CS + (u32)((t < 4 ? 64 * wn + 16 * t : 128) + r16) * 4u;
                    cs[t] = lds_ld<float>(ca);
                }
                const float sgn = (j >> 2) ? -1.f : 1.f;
                const u32 dst = WS_AB + (u32)((j * 4 + (mb & 3)) * 16 + 8 * (mb >> 2)) * 9216u + l16;
#pragma unroll
                for (int Pi = 0; Pi < 2; ++Pi) {
                    const int sb = 64 * wm + 32 * ((Pi + wn) & 1);
                    float mean[8], rs[8];
#pragma unroll
                    for (int e = 0; e < 4; ++e) {
                        f32x4 st = lds_ld<f32x4>(L_STAT + (sb + 8 * q + 2 * e) * 8);
                        mean[2 * e] = st[0]; rs[2 * e] = st[1] * sgn; mean[2 * e + 1] = st[2]; rs[2 * e + 1] = st[3] * sgn;
                    }
#pragma unroll
                    for (int t = 0; t < 5; ++t) {
                        if (t == 4 && Pi == 1) break;
                        f32x4 lo = (t < 4) ? acc[2 * Pi][t & 3] : acc8[0], hi = (t < 4) ? acc[2 * Pi + 1][t & 3] : acc8[1];
                        float v[8];
#pragma unroll
                        for (int e = 0; e < 4; ++e) { v[e] = (lo[e] - mean[e] * cs[t]) * rs[e]; v[4 + e] = (hi[e] - mean[4 + e] * cs[t]) * rs[4 + e]; }
                        u32x4 o = {pk2(v[0], v[1]), pk2(v[2], v[3]), pk2(v[4], v[5]), pk2(v[6], v[7])};
                        const int nf = (t < 4) ? 4 * wn + t : 8;
                        __builtin_amdgcn_raw_buffer_store_b128(o, rs_ws, dst + (u32)((sb >> 5) * 9 + nf) * 1024u, 0, 16);
                    }
                }
            }
            tag = __builtin_amdgcn_readfirstlane(lds_ld<u32>(L_TAG));
            WAIT_V(0);
            if (l == 0) st_flag(ws + WS_FAB + ((j * 8 + mb) * 8 + w) * 4, tag);
        }
        {
            const u32 wA = (u32)wm * 8192u + (u32)wn * 4096u, wB = L_B + (u32)wn * 4096u;
            const u32 trb = (u32)(l >> 5) * 2048u + trl, trs = (u32)(l >> 5) * 512u + (u32)(l & 31) * 8u;
            f32x4 acc[4][4], acc8[2];
#pragma unroll
            for (int i = 0; i < 4; ++i)
#pragma unroll
                for (int t = 0; t < 4; ++t) { acc[i][t] = (f32x4){0.f, 0.f, 0.f, 0.f}; PIN(acc[i][t]); }
            acc8[0] = acc8[1] = (f32x4){0.f, 0.f, 0.f, 0.f}; PIN(acc8[0]); PIN(acc8[1]);
            u32 a01, a23, b0, b1, b8, st;
            u32 sA = wA, sS = L_B + 8192u + 36864u, sB = wB + 36864u, sT = (u32)(-c0);
            const u32 sD = (u32)wn * 4096u - 8192u, sE = 17u - c0;
            WAIT_L(0); BAR(); SB();
            STAMP(3);
            asm volatile(
                "s_cmp_lt_u32 %[sT], 8\n\t"
                "s_cbranch_scc1 LPC%=\n\t"
                "v_add_u32 %[a01], %[sA], %[l16]\n\t"
                "v_xor_b32 %[a23], 0x1000, %[a01]\n\t"
                "v_add_u32 %[b0], %[sB], %[trb]\n\t"
                "v_xor_b32 %[b1], 32, %[b0]\n\t"
                "v_add_u32 %[b8], %[sS], %[trs]\n\t"
                "ds_read_b128 v[96:99], %[a01] offset:0\n\t"
                "ds_read_b128 v[100:103], %[a01] offset:2048\n\t"
                "ds_read_b128 v[104:107], %[a23] offset:0\n\t"
                "ds_read_b128 v[108:111], %[a23] offset:2048\n\t"
                "ds_read_b64_tr_b16 v[128:129], %[b0] offset:0\n\t"
                "ds_read_b64_tr_b16 v[130:131], %[b0] offset:1024\n\t"
                "ds_read_b64_tr_b16 v[132:133], %[b1] offset:0\n\t"
                "ds_read_b64_tr_b16 v[134:135], %[b1] offset:1024\n\t"
                "ds_read_b64_tr_b16 v[136:137], %[b0] offset:512\n\t"
                "ds_read_b64_tr_b16 v[138:139], %[b0] offset:1536\n\t"
                "ds_read_b64_tr_b16 v[140:141], %[b1] offset:512\n\t"
                "ds_read_b64_tr_b16 v[142:143], %[b1] offset:1536\n\t"
                "ds_read_b64_tr_b16 v[144:145], %[b8] offset:0\n\t"
                "ds_read_b64_tr_b16 v[146:147], %[b8] offset:256\n\t"
                "s_branch LH0%=\n\t"
                "LPC%=:\n\t"
                "v_add_u32 %[a01], %[sA], %[rowl]\n\t"
                "v_xor_b32 %[a23], 0x1000, %[a01]\n\t"
                "v_add_u32 %[b0], %[sB], %[l16]\n\t"
                "v_add_u32 %[b8], %[sS], %[l16]\n\t"
                "ds_read_b128 v[96:99], %[a01] offset:0\n\t"
                "ds_read_b128 v[100:103], %[a01] offset:2048\n\t"
                "ds_read_b128 v[104:107], %[a23] offset:0\n\t"
                "ds_read_b128 v[108:111], %[a23] offset:2048\n\t"
                "ds_read_b128 v[128:131], %[b0] offset:0\n\t"
                "ds_read_b128 v[132:135], %[b0] offset:1024\n\t"
                "ds_read_b128 v[136:139], %[b0] offset:2048\n\t"
                "ds_read_b128 v[140:143], %[b0] offset:3072\n\t"
                "ds_read_b128 v[144:147], %[b8] offset:0\n\t"
                "s_branch LC0%=\n\t"
                "LC1%=:\n\t"
                "v_mfma_f32_16x16x32_bf16 %0, v[148:151], v[112:115], %0\n\t"
                "v_add_u32 %[a01], %[sA], %[rowl]\n\t"
                "v_xor_b32 %[a23], 0x1000, %[a01]\n\t"
                "ds_read_b128 v[96:99], %[a01] offset:0\n\t"
                "v_mfma_f32_16x16x32_bf16 %4, v[148:151], v[116:119], %4\n\t"
                "v_add_u32 %[b0], %[sB], %[l16]\n\t"
                "v_add_u32 %[b8], %[sS], %[l16]\n\t"
                "ds_read_b128 v[100:103], %[a01] offset:2048\n\t"
                "v_mfma_f32_16x16x32_bf16 %8, v[148:151], v[120:123], %8\n\t"
                "ds_read_b128 v[104:107], %[a23] offset:0\n\t"
                "v_mfma_f32_16x16x32_bf16 %12, v[148:151], v[124:127], %12\n\t"
                "ds_read_b128 v[108:111], %[a23] offset:2048\n\t"
                "v_mfma_f32_16x16x32_bf16 %1, v[152:155], v[112:115], %1\n\t"
                "ds_read_b128 v[128:131], %[b0] offset:0\n\t"
                "v_mfma_f32_16x16x32_bf16 %5, v[152:155], v[116:119], %5\n\t"
                "ds_read_b128 v[132:135], %[b0] offset:1024\n\t"
                "v_mfma_f32_16x16x32_bf16 %9, v[152:155], v[120:123], %9\n\t"
                "ds_read_b128 v[136:139], %[b0] offset:2048\n\t"
                "v_mfma_f32_16x16x32_bf16 %13, v[152:155], v[124:127], %13\n\t"
                "ds_read_b128 v[140:143], %[b0] offset:3072\n\t"
                "v_mfma_f32_16x16x32_bf16 %2, v[156:159], v[112:115], %2\n\t"
                "ds_read_b128 v[144:147], %[b8] offset:0\n\t"
                "v_mfma_f32_16x16x32_bf16 %6, v[156:159], v[116:119], %6\n\t"
                "v_mfma_f32_16x16x32_bf16 %10, v[156:159], v[120:123], %10\n\t"
                "v_mfma_f32_16x16x32_bf16 %14, v[156:159], v[124:127], %14\n\t"
                "v_mfma_f32_16x16x32_bf16 %3, v[160:163], v[112:115], %3\n\t"
                "v_mfma_f32_16x16x32_bf16 %7, v[160:163], v[116:119], %7\n\t"
                "v_mfma_f32_16x16x32_bf16 %11, v[160:163], v[120:123], %11\n\t"
                "v_mfma_f32_16x16x32_bf16 %15, v[160:163], v[124:127], %15\n\t"
                "v_mfma_f32_16x16x32_bf16 %16, v[164:167], v[112:115], %16\n\t"
                "v_mfma_f32_16x16x32_bf16 %17, v[164:167], v[116:119], %17\n\t"
                "LC0%=:\n\t"
                "s_waitcnt lgkmcnt(0)\n\t"
                "v_mfma_f32_16x16x32_bf16 %0, v[128:131], v[96:99], %0\n\t"
                "v_xor_b32 %[a01], 64, %[a01]\n\t"
                "v_xor_b32 %[a23], 64, %[a23]\n\t"
                "ds_read_b128 v[112:115], %[a01] offset:0\n\t"
                "v_mfma_f32_16x16x32_bf16 %4, v[128:131], v[100:103], %4\n\t"
                "ds_read_b128 v[116:119], %[a01] offset:2048\n\t"
                "v_mfma_f32_16x16x32_bf16 %8, v[128:131], v[104:107], %8\n\t"
                "ds_read_b128 v[120:123], %[a23] offset:0\n\t"
                "v_mfma_f32_16x16x32_bf16 %12, v[128:131], v[108:111], %12\n\t"
                "ds_read_b128 v[124:127], %[a23] offset:2048\n\t"
                "v_mfma_f32_16x16x32_bf16 %1, v[132:135], v[96:99], %1\n\t"
                "ds_read_b128 v[148:151], %[b0] offset:9216\n\t"
                "v_mfma_f32_16x16x32_bf16 %5, v[132:135], v[100:103], %5\n\t"
                "ds_read_b128 v[152:155], %[b0] offset:10240\n\t"
                "v_mfma_f32_16x16x32_bf16 %9, v[132:135], v[104:107], %9\n\t"
                "ds_read_b128 v[156:159], %[b0] offset:11264\n\t"
                "v_mfma_f32_16x16x32_bf16 %13, v[132:135], v[108:111], %13\n\t"
                "ds_read_b128 v[160:163], %[b0] offset:12288\n\t"
                "v_mfma_f32_16x16x32_bf16 %2, v[136:139], v[96:99], %2\n\t"
                "ds_read_b128 v[164:167], %[b8] offset:9216\n\t"
                "v_mfma_f32_16x16x32_bf16 %6, v[136:139], v[100:103], %6\n\t"
                "v_mfma_f32_16x16x32_bf16 %10, v[136:139], v[104:107], %10\n\t"
                "s_add_u32 %[sA], %[sA], 0x8000\n\t"
                "s_cmp_lt_u32 %[sA], 0x18000\n\t"
                "s_cselect_b32 %[st], 0, 0x18000\n\t"
                "s_sub_u32 %[sA], %[sA], %[st]\n\t"
                "v_mfma_f32_16x16x32_bf16 %14, v[136:139], v[108:111], %14\n\t"
                "s_add_u32 %[sS], %[sS], 18432\n\t"
                "s_cmp_lt_u32 %[sS], 161792\n\t"
                "s_cselect_b32 %[st], 0, 55296\n\t"
                "s_sub_u32 %[sS], %[sS], %[st]\n\t"
                "v_mfma_f32_16x16x32_bf16 %3, v[140:143], v[96:99], %3\n\t"
                "s_add_u32 %[sB], %[sS], %[sD]\n\t"
                "s_add_u32 %[sT], %[sT], 1\n\t"
                "s_cmp_lt_u32 %[sT], 8\n\t"
                "v_mfma_f32_16x16x32_bf16 %7, v[140:143], v[100:103], %7\n\t"
                "v_mfma_f32_16x16x32_bf16 %11, v[140:143], v[104:107], %11\n\t"
                "v_mfma_f32_16x16x32_bf16 %15, v[140:143], v[108:111], %15\n\t"
                "v_mfma_f32_16x16x32_bf16 %16, v[144:147], v[96:99], %16\n\t"
                "v_mfma_f32_16x16x32_bf16 %17, v[144:147], v[100:103], %17\n\t"
                "s_waitcnt lgkmcnt(0)\n\t"
                "s_barrier\n\t"
                "s_cbranch_scc1 LC1%=\n\t"
                "LH1%=:\n\t"
                "v_mfma_f32_16x16x32_bf16 %0, v[148:151], v[112:115], %0\n\t"
                "v_add_u32 %[a01], %[sA], %[l16]\n\t"
                "v_xor_b32 %[a23], 0x1000, %[a01]\n\t"
                "ds_read_b128 v[96:99], %[a01] offset:0\n\t"
                "v_mfma_f32_16x16x32_bf16 %4, v[148:151], v[116:119], %4\n\t"
                "v_add_u32 %[b0], %[sB], %[trb]\n\t"
                "v_xor_b32 %[b1], 32, %[b0]\n\t"
                "v_add_u32 %[b8], %[sS], %[trs]\n\t"
                "ds_read_b128 v[100:103], %[a01] offset:2048\n\t"
                "v_mfma_f32_16x16x32_bf16 %8, v[148:151], v[120:123], %8\n\t"
                "ds_read_b128 v[104:107], %[a23] offset:0\n\t"
                "v_mfma_f32_16x16x32_bf16 %12, v[148:151], v[124:127], %12\n\t"
                "ds_read_b128 v[108:111], %[a23] offset:2048\n\t"
                "v_mfma_f32_16x16x32_bf16 %1, v[152:155], v[112:115], %1\n\t"
                "ds_read_b64_tr_b16 v[128:129], %[b0] offset:0\n\t"
                "ds_read_b64_tr_b16 v[130:131], %[b0] offset:1024\n\t"
                "v_mfma_f32_16x16x32_bf16 %5, v[152:155], v[116:119], %5\n\t"
                "ds_read_b64_tr_b16 v[132:133], %[b1] offset:0\n\t"
                "ds_read_b64_tr_b16 v[134:135], %[b1] offset:1024\n\t"
                "v_mfma_f32_16x16x32_bf16 %9, v[152:155], v[120:123], %9\n\t"
                "ds_read_b64_tr_b16 v[136:137], %[b0] offset:512\n\t"
                "ds_read_b64_tr_b16 v[138:139], %[b0] offset:1536\n\t"
                "v_mfma_f32_16x16x32_bf16 %13, v[152:155], v[124:127], %13\n\t"
                "ds_read_b64_tr_b16 v[140:141], %[b1] offset:512\n\t"
                "ds_read_b64_tr_b16 v[142:143], %[b1] offset:1536\n\t"
                "v_mfma_f32_16x16x32_bf16 %2, v[156:159], v[112:115], %2\n\t"
                "ds_read_b64_tr_b16 v[144:145], %[b8] offset:0\n\t"
                "ds_read_b64_tr_b16 v[146:147], %[b8] offset:256\n\t"
                "v_mfma_f32_16x16x32_bf16 %6, v[156:159], v[116:119], %6\n\t"
                "v_mfma_f32_16x16x32_bf16 %10, v[156:159], v[120:123], %10\n\t"
                "v_mfma_f32_16x16x32_bf16 %14, v[156:159], v[124:127], %14\n\t"
                "v_mfma_f32_16x16x32_bf16 %3, v[160:163], v[112:115], %3\n\t"
                "v_mfma_f32_16x16x32_bf16 %7, v[160:163], v[116:119], %7\n\t"
                "v_mfma_f32_16x16x32_bf16 %11, v[160:163], v[120:123], %11\n\t"
                "v_mfma_f32_16x16x32_bf16 %15, v[160:163], v[124:127], %15\n\t"
                "v_mfma_f32_16x16x32_bf16 %16, v[164:167], v[112:115], %16\n\t"
                "v_mfma_f32_16x16x32_bf16 %17, v[164:167], v[116:119], %17\n\t"
                "s_cmp_eq_u32 %[sT], %[sE]\n\t"
                "s_cbranch_scc1 LEND%=\n\t"
                "LH0%=:\n\t"
                "s_waitcnt lgkmcnt(0)\n\t"
                "v_mfma_f32_16x16x32_bf16 %0, v[128:131], v[96:99], %0\n\t"
                "ds_read_b128 v[112:115], %[a01] offset:1024\n\t"
                "v_mfma_f32_16x16x32_bf16 %4, v[128:131], v[100:103], %4\n\t"
                "ds_read_b128 v[116:119], %[a01] offset:3072\n\t"
                "v_mfma_f32_16x16x32_bf16 %8, v[128:131], v[104:107], %8\n\t"
                "ds_read_b128 v[120:123], %[a23] offset:1024\n\t"
                "v_mfma_f32_16x16x32_bf16 %12, v[128:131], v[108:111], %12\n\t"
                "ds_read_b128 v[124:127], %[a23] offset:3072\n\t"
                "v_mfma_f32_16x16x32_bf16 %1, v[132:135], v[96:99], %1\n\t"
                "ds_read_b64_tr_b16 v[148:149], %[b0] offset:9216\n\t"
                "ds_read_b64_tr_b16 v[150:151], %[b0] offset:10240\n\t"
                "v_mfma_f32_16x16x32_bf16 %5, v[132:135], v[100:103], %5\n\t"
                "ds_read_b64_tr_b16 v[152:153], %[b1] offset:9216\n\t"
                "ds_read_b64_tr_b16 v[154:155], %[b1] offset:10240\n\t"
                "v_mfma_f32_16x16x32_bf16 %9, v[132:135], v[104:107], %9\n\t"
                "ds_read_b64_tr_b16 v[156:157], %[b0] offset:9728\n\t"
                "ds_read_b64_tr_b16 v[158:159], %[b0] offset:10752\n\t"
                "v_mfma_f32_16x16x32_bf16 %13, v[132:135], v[108:111], %13\n\t"
                "ds_read_b64_tr_b16 v[160:161], %[b1] offset:9728\n\t"
                "ds_read_b64_tr_b16 v[162:163], %[b1] offset:10752\n\t"
                "v_mfma_f32_16x16x32_bf16 %2, v[136:139], v[96:99], %2\n\t"
                "ds_read_b64_tr_b16 v[164:165], %[b8] offset:9216\n\t"
                "ds_read_b64_tr_b16 v[166:167], %[b8] offset:9472\n\t"
                "v_mfma_f32_16x16x32_bf16 %6, v[136:139], v[100:103], %6\n\t"
                "v_mfma_f32_16x16x32_bf16 %10, v[136:139], v[104:107], %10\n\t"
                "s_add_u32 %[sA], %[sA], 0x8000\n\t"
                "s_cmp_lt_u32 %[sA], 0x18000\n\t"
                "s_cselect_b32 %[st], 0, 0x18000\n\t"
                "s_sub_u32 %[sA], %[sA], %[st]\n\t"
                "v_mfma_f32_16x16x32_bf16 %14, v[136:139], v[108:111], %14\n\t"
                "s_add_u32 %[sS], %[sS], 18432\n\t"
                "s_cmp_lt_u32 %[sS], 161792\n\t"
                "s_cselect_b32 %[st], 0, 55296\n\t"
                "s_sub_u32 %[sS], %[sS], %[st]\n\t"
                "v_mfma_f32_16x16x32_bf16 %3, v[140:143], v[96:99], %3\n\t"
                "s_add_u32 %[sB], %[sS], %[sD]\n\t"
                "s_add_u32 %[sT], %[sT], 1\n\t"
                "s_cmp_lt_u32 %[sT], 8\n\t"
                "v_mfma_f32_16x16x32_bf16 %7, v[140:143], v[100:103], %7\n\t"
                "v_mfma_f32_16x16x32_bf16 %11, v[140:143], v[104:107], %11\n\t"
                "v_mfma_f32_16x16x32_bf16 %15, v[140:143], v[108:111], %15\n\t"
                "v_mfma_f32_16x16x32_bf16 %16, v[144:147], v[96:99], %16\n\t"
                "v_mfma_f32_16x16x32_bf16 %17, v[144:147], v[100:103], %17\n\t"
                "s_waitcnt lgkmcnt(0)\n\t"
                "s_barrier\n\t"
                "s_cbranch_scc0 LH1%=\n\t"
                "s_branch LC1%=\n\t"
                "LEND%=:\n\t"
                "s_waitcnt lgkmcnt(0)\n\t"
                "s_nop 7\n\t"
                : "+v"(acc[0][0]), "+v"(acc[0][1]), "+v"(acc[0][2]), "+v"(acc[0][3]), "+v"(acc[1][0]), "+v"(acc[1][1]), "+v"(acc[1][2]), "+v"(acc[1][3]), "+v"(acc[2][0]), "+v"(acc[2][1]), "+v"(acc[2][2]), "+v"(acc[2][3]), "+v"(acc[3][0]), "+v"(acc[3][1]), "+v"(acc[3][2]), "+v"(acc[3][3]), "+v"(acc8[0]), "+v"(acc8[1]),
                  [a01] "=&v"(a01), [a23] "=&v"(a23), [b0] "=&v"(b0), [b1] "=&v"(b1), [b8] "=&v"(b8),
                  [sA] "+s"(sA), [sB] "+s"(sB), [sS] "+s"(sS), [sT] "+s"(sT), [st] "=&s"(st)
                : [l16] "v"(l16), [rowl] "v"(rowl), [trb] "v"(trb), [trs] "v"(trs), [sD] "s"(sD), [sE] "s"(sE)
                : "memory", "scc", "v96", "v97", "v98", "v99", "v100", "v101", "v102", "v103", "v104", "v105", "v106", "v107", "v108", "v109", "v110", "v111", "v112", "v113", "v114", "v115", "v116", "v117", "v118", "v119", "v120", "v121", "v122", "v123", "v124", "v125", "v126", "v127", "v128", "v129", "v130", "v131", "v132", "v133", "v134", "v135", "v136", "v137", "v138", "v139", "v140", "v141", "v142", "v143", "v144", "v145", "v146", "v147", "v148", "v149", "v150", "v151", "v152", "v153", "v154", "v155", "v156", "v157", "v158", "v159", "v160", "v161", "v162", "v163", "v164", "v165", "v166", "v167");
            STAMP(4);
            const u32 pd = WS_P + (u32)((g * 8 + w) * 72 + j) * 1024u + l16;
#pragma unroll
            for (int b = 0; b < 9; ++b) {
                f32x4 lo = (b < 8) ? acc[2 * (b >> 2)][b & 3] : acc8[0], hi = (b < 8) ? acc[2 * (b >> 2) + 1][b & 3] : acc8[1];
                u32x4 o = {pk2(lo[0], lo[1]), pk2(lo[2], lo[3]), pk2(hi[0], hi[1]), pk2(hi[2], hi[3])};
                if (w == j) lds_st<u32x4>(b * 1024u + l16, o);
                else __builtin_amdgcn_raw_buffer_store_b128(o, rs_ws, pd + b * 8192u, 0, 16);
            }
            WAIT_L(0); BAR();
            if (w != j) {
                WAIT_V(0);
                if (l == 0) st_flag(ws + WS_FP + ((g * 8 + w) * 8 + j) * 4, tag);
            }
            STAMP(5);
        }
    } else if (w < 10) {
        const int ld = w - 8;
        const rsrc_t rs_x = mk_rsrc(x, S_ * D_ * 2);
        const u32 sw0 = 16u * ((l & 7) ^ q), sw1 = sw0 ^ 64u;
        const rsrc_t rs_c = mk_rsrc((j >> 2) ? Ci : Cr, S_ * S_ * 2);
        const rsrc_t rs_w2 = mk_rsrc(w2, F_ * D_ * 2);
        const u32 vC = (u32)(l >> 3) * 4096u;
        const u32 sC = (u32)(256 * mb + 128 * ld) * 4096u + 1024u * (j & 3);
        const int rot = 2 * nb + mb;
        auto issueC = [&](int t) {
            const u32 lb = UNI((u32)(t % 3) * 32768u + ld * 16384u), so = UNI(sC + 128u * ((t - c0 + rot) & 7));
#pragma unroll
            for (int i = 0; i < 16; ++i) dma<0>(rs_c, lb + i * 1024u, vC + ((i & 1) ? sw1 : sw0), so + i * 32768u);
        };
        auto ksof = [&](int t) -> int { int k = is_ab ? t - 8 : (t < 3 ? 3 * nb + t : 3 * nb + t - 8); return k >= 9 ? k - 9 : k; };
        auto issueH = [&](int t) {
            const u32 lb = UNI((u32)(t % 3) * 32768u + ld * 16384u), so = UNI(WS_H + (u32)((j * 8 + mb) * 9 + ksof(t)) * 32768u + ld * 16384u);
#pragma unroll
            for (int i = 0; i < 16; ++i) dma<16>(rs_ws, lb + i * 1024u, l16, so + i * 1024u);
        };
        const u32 a8 = (l >> 2) & 7;
        const u32 vW = a8 * 1152u + 32u * (((l >> 1) & 1) ^ (a8 >> 2)) + 16u * (l & 1);
        const u32 vW0 = vW + 2u * colmap(nb, 2 * (l >> 5)), vW1 = vW + 2u * colmap(nb, 4 + 2 * (l >> 5));
        const u32 vWs = (u32)(16 * ((l >> 4) & 1) + 8 * (l >> 5) + ((l >> 1) & 7)) * 1152u + 2u * colmap(nb, 8) + 16u * (l & 1);
        auto issueW = [&](int t) {
            const u32 lb = UNI(L_B + (u32)((t + 2) % 3) * 18432u + ld * 9216u), so = UNI((u32)(576 * j + 64 * ksof(t) + 32 * ld) * 1152u + 288u * nb);
#pragma unroll
            for (int i = 0; i < 8; ++i) dma<0>(rs_w2, lb + i * 1024u, (i >> 2) ? vW1 : vW0, so + (u32)(16 * (i & 1) + 8 * ((i >> 1) & 1)) * 1152u);
            dma<0>(rs_w2, lb + 8192u, vWs, so);
        };
        auto issueAB = [&](int t) {
            const u32 lb = UNI(L_B + (u32)((t + 2) % 3) * 18432u + ld * 9216u), so = UNI(WS_AB + (u32)((j * 4 + nb) * 16 + 2 * ((t - c0 + rot) & 7) + ld) * 9216u);
#pragma unroll
            for (int i = 0; i < 9; ++i) dma<16>(rs_ws, lb + i * 1024u, l16, so + i * 1024u);
        };
        const char* fab = ws + WS_FAB + ((j * 8 + nb + 4 * ((l >> 3) & 1)) * 8 + (l & 7)) * 4;
        const char* fhp = ws + WS_FH + ((j * 8 + mb) * 8 + (r16 % 6)) * 4;
        u32 fv = 0;
        if (!is_ab) {
            const rsrc_t rs_w1 = mk_rsrc(w1, D_ * F_ * 2);
            const u32 vA = (u32)(l >> 3) * 1152u, sA = (u32)(row0 + 128 * ld) * 1152u;
            const u32 vB = (8u * (a8 >> 2) + (a8 & 3)) * 9216u + (u32)(l >> 5) * 64u + 32u * (((l >> 1) & 1) ^ (a8 >> 2)) + 16u * (l & 1);
            const u32 sB = (u32)(16 * ld) * 9216u + (u32)(576 * j + 192 * nb) * 2u;
            u32 bsl = L_B + ld * 6144u;
            auto issueB = [&](int u) {
                const u32 so = sB + (u32)u * (32u * 9216u);
#pragma unroll
                for (int i = 0; i < 6; ++i) dma<0>(rs_w1, bsl + i * 1024u, vB, so + (u32)(i / 3) * (4u * 9216u) + (u32)(i % 3) * 128u);
                bsl = (bsl >= L_B + 49152u) ? bsl - 49152u : bsl + 12288u;
            };
#pragma unroll 1
            for (int s = -3; s <= 6; ++s) {
                if (s == 0) { WAIT_V(50); ABAR(); STAMP(1); }
                if (s >= 0) { WAIT_V(22); ABAR(); }
                if (s > -3) issueB(2 * s + 5);
                if (s < 6) {
                    issueB(2 * s + 6);
                    const u32 lb = (u32)((s + 3) % 3) * 32768u + ld * 16384u, so = sA + (u32)(s + 3) * 128u;
#pragma unroll
                    for (int i = 0; i < 16; ++i) dma<0>(rs_x, lb + i * 1024u, vA + ((i & 1) ? sw1 : sw0), so + i * (8u * 1152u));
                }
            }
            WAIT_V(0); ABAR();
            issueW(0);
            ABAR();
            issueW(1); issueW(2);
            tag = __builtin_amdgcn_readfirstlane(lds_ld<u32>(L_TAG));
            WAIT_V(18); ABAR();
            STAMP(2);
            WAIT_V(9); ABAR();
            asm volatile("global_load_dword %0, %1, off sc1" : "=v"(fv) : "v"(fab) : "memory");
            issueC(3);
            asm volatile("s_waitcnt vmcnt(16)" : "+v"(fv) :: "memory");
            ABAR();
            STAMP(3);
            if (!__all(fv == tag)) poll(fab, tag);
            STAMP(4);
            issueAB(3);
        } else {
            const rsrc_t rs_d = mk_rsrc((j >> 2) ? Di : Dr, D_ * D_ * 2);
            const u32 vA = (u32)(8 * (l >> 5) + ((l >> 3) & 3)) * 1152u, sA = (u32)(row0 + 128 * ld) * 1152u;
            const u32 vB = (u32)(l >> 3) * 1152u;
            const int cn = mb & 3;
#pragma unroll 1
            for (int s = -3; s <= 5; ++s) {
                if (s == 0) { WAIT_V(50); ABAR(); STAMP(1); }
                if (s >= 0) { WAIT_V(25); ABAR(); }
                const u32 sl = (u32)((s + 3) % 3);
                const u32 lb = sl * 32768u + ld * 16384u, so = sA + (u32)(s + 3) * 128u;
#pragma unroll
                for (int i = 0; i < 16; ++i)
                    dma<0>(rs_x, lb + i * 1024u, vA + ((i & 1) ? sw1 : sw0), so + (u32)(32 * (i >> 2) + 16 * (i & 1) + 4 * ((i >> 1) & 1)) * 1152u);
                const u32 lbb = L_B + sl * 18432u;
#pragma unroll
                for (int i = 0; i < 9; ++i)
                    dma<0>(rs_d, lbb + (2 * i + ld) * 1024u, vB + (ld ? sw1 : sw0), (144u * cn + colmap(cn, i) + 8u * ld) * 1152u + (u32)(s + 3) * 128u);
            }
            WAIT_V(25); ABAR();
            issueC(0);
            WAIT_V(16); ABAR();
            issueC(1);
            ABAR();
            issueC(2);
            tag = __builtin_amdgcn_readfirstlane(lds_ld<u32>(L_TAG));
            STAMP(3);
            poll(fab, tag);
            STAMP(4);
            issueAB(0); issueAB(1); issueAB(2);
            WAIT_V(18); ABAR();
            STAMP(2);
            WAIT_V(9); ABAR();
            issueC(3); issueAB(3);
            WAIT_V(25); ABAR();
        }
#pragma unroll 1
        for (int t = 4; t <= 16; ++t) {
            if (t == c0 + 7) asm volatile("global_load_dword %0, %1, off sc1" : "=v"(fv) : "v"(fhp) : "memory");
            if (t == c0 + 8) { STAMP(5); if (!__all(fv == tag)) poll(fhp, tag); STAMP(6); }
            if (t < c0 + 8) { issueC(t); issueAB(t); } else { issueH(t); issueW(t); }

            asm volatile("s_waitcnt vmcnt(25)" : "+v"(fv) :: "memory");

            ABAR();

        }
        WAIT_V(0); ABAR();
        ABAR();
        ABAR();
    } else {
        const int ax = w - 10;
        struct Row { u32x4 v[4]; u32x2 t; };
        auto rowload = [&](rsrc_t rs, u32 off) -> Row {
            Row r;
#pragma unroll
            for (int i = 0; i < 4; ++i) r.v[i] = __builtin_amdgcn_raw_buffer_load_b128(rs, off + r16 * 16 + 256 * i, 0, 0);
            r.t = __builtin_amdgcn_raw_buffer_load_b64(rs, off + r16 * 8 + 1024, 0, 0);
            return r;
        };
        auto rowsum = [&](const Row& r) -> float {
            f32x2 s2 = add2(r.t[1], add2(r.t[0], (f32x2){0.f, 0.f}));
#pragma unroll
            for (int i = 0; i < 16; ++i) s2 = add2(r.v[i >> 2][i & 3], s2);
            return row16_sum(s2[0] + s2[1]);
        };
        const u32 epoch = __builtin_amdgcn_raw_buffer_load_b32(rs_ws, WS_EPOCH, 0, 16);
        const Row xr = rowload(mk_rsrc(x, S_ * D_ * 2), (u32)(bid * 8 + 4 * ax + q) * 1152u);
        auto stats = [&]() {
            tag = __builtin_amdgcn_readfirstlane(epoch) + 1u;
            const float mean = rowsum(xr) * (1.f / D_);
            const f32x2 nm = {-mean, -mean};
            f32x2 ss = {0.f, 0.f};
#pragma unroll
            for (int i = 0; i < 18; ++i) { f32x2 d = add2(i < 16 ? xr.v[(i >> 2) & 3][i & 3] : xr.t[i & 1], nm); ss = __builtin_elementwise_fma(d, d, ss); }
            const float rstd = 1.0f / sqrtf(row16_sum(ss[0] + ss[1]) * (1.f / D_) + 1e-5f);
            u32x4 gr = {__float_as_uint(mean), tag, __float_as_uint(rstd), tag};
            if (r16 == 0) __builtin_amdgcn_raw_buffer_store_b128(gr, rs_ws, WS_STAT + (bid * 8 + 4 * ax + q) * 16, 0, 16);
            if (ax == 0) lds_st<u32>(L_TAG, tag);
        };
        u32x4 sg[2];
        const u32 sgo = WS_STAT + (u32)(row0 + 128 * ax + l) * 16u;
        auto sg_load = [&]() { sg[0] = __builtin_amdgcn_raw_buffer_load_b128(rs_ws, sgo, 0, 16); sg[1] = __builtin_amdgcn_raw_buffer_load_b128(rs_ws, sgo + 1024, 0, 16); };
        auto sg_done = [&]() {
#pragma unroll 1
            for (int it = 0; it < SPIN_MAX; ++it) {
                if (__all(sg[0][1] == tag && sg[0][3] == tag && sg[1][1] == tag && sg[1][3] == tag)) break;
                __builtin_amdgcn_s_sleep(1);
                sg_load();
            }
            lds_st<u32x2>(L_STAT + (128 * ax + l) * 8, (u32x2){sg[0][0], sg[0][2]});
            lds_st<u32x2>(L_STAT + (128 * ax + 64 + l) * 8, (u32x2){sg[1][0], sg[1][2]});
        };
        ABAR();
        STAMP(1);
        if (!is_ab) {
            u32x4 wv[15];
            {
                const rsrc_t rs_w1 = mk_rsrc(w1, D_ * F_ * 2);
                const u32 vo = (u32)(72 * mb + 40 * ax + (l >> 3)) * 9216u + (u32)(576 * j + 192 * nb) * 2u + 16u * (l & 7);
#pragma unroll
                for (int i = 0; i < 15; ++i)
                    wv[i] = __builtin_amdgcn_raw_buffer_load_b128(rs_w1, (i >= 12 && ax) ? 0x80000000u : vo + (u32)(i / 3) * (8u * 9216u) + (u32)(i % 3) * 128u, 0, 0);
            }
            stats();
            ABAR();
            f32x2 c2[12] = {};
#pragma unroll
            for (int i = 0; i < 15; ++i) {
#pragma unroll
                for (int e = 0; e < 4; ++e) c2[(i % 3) * 4 + e] = add2(wv[i][e], c2[(i % 3) * 4 + e]);
                if (i == 6 || i == 14) {
#pragma unroll
                    for (int e = 0; e < 12; ++e) PIN(c2[e]);
                    ABAR();
                }
            }
            float c3[3];
            {
                float c12[12], c6[6];
#pragma unroll
                for (int v = 0; v < 12; ++v) {
                    auto sw = __builtin_amdgcn_permlane32_swap(__float_as_uint(c2[v >> 1][v & 1]), __float_as_uint(c2[6 + (v >> 1)][v & 1]), false, false);
                    c12[v] = __uint_as_float(sw[0]) + __uint_as_float(sw[1]);
                }
#pragma unroll
                for (int v = 0; v < 6; ++v) {
                    auto sw = __builtin_amdgcn_permlane16_swap(__float_as_uint(c12[v]), __float_as_uint(c12[6 + v]), false, false);
                    c6[v] = __uint_as_float(sw[0]) + __uint_as_float(sw[1]);
                }
#pragma unroll
                for (int v = 0; v < 3; ++v) { float lo = dpp_add<0x128>(c6[v]), hi = dpp_add<0x128>(c6[3 + v]); c3[v] = (l & 8) ? hi : lo; }
            }
            const u32 cpo = WS_CP + (u32)((j * 3 + nb) * 16) * 1536u;
            {
                const u32 o = cpo + (u32)(2 * mb + ax) * 1536u;
                __builtin_amdgcn_raw_buffer_store_b128((u32x4){__float_as_uint(c3[0]), tag, __float_as_uint(c3[1]), tag}, rs_ws, o + l16, 0, 16);
                __builtin_amdgcn_raw_buffer_store_b64((u32x2){__float_as_uint(c3[2]), tag}, rs_ws, o + 1024 + l * 8, 0, 16);
            }
            ABAR();
            sg_load();
            ABAR();
            sg_done();
            ABAR();
            u32x4 pa4[8]; u32x2 pa2[8];
            auto pload = [&]() {
#pragma unroll
                for (int k = 0; k < 8; ++k) {
                    pa4[k] = __builtin_amdgcn_raw_buffer_load_b128(rs_ws, cpo + (u32)(8 * ax + k) * 1536u + l16, 0, 16);
                    pa2[k] = __builtin_amdgcn_raw_buffer_load_b64(rs_ws, cpo + (u32)(8 * ax + k) * 1536u + 1024 + l * 8, 0, 16);
                }
            };
            pload();
            ABAR();
#pragma unroll 1
            for (int it = 0; it < SPIN_MAX; ++it) {
                bool ok = true;
#pragma unroll
                for (int k = 0; k < 8; ++k) ok = ok && pa4[k][1] == tag && pa4[k][3] == tag && pa2[k][1] == tag;
                if (__all(ok)) break;
                __builtin_amdgcn_s_sleep(1);
                pload();
            }
            {
                float t0 = 0.f, t1 = 0.f, t2 = 0.f;
#pragma unroll
                for (int k = 0; k < 8; ++k) { t0 += __uint_as_float(pa4[k][0]); t1 += __uint_as_float(pa4[k][2]); t2 += __uint_as_float(pa2[k][0]); }
                const int v0 = 12 * (l >> 5) + 6 * ((l >> 4) & 1) + 3 * ((l >> 3) & 1);
                auto ca = [&](int v) -> u32 { return L_CS + 768u * ax + (u32)(64 * (v >> 3) + 8 * (l & 7) + (v & 7)) * 4u; };
                lds_st<float>(ca(v0), t0); lds_st<float>(ca(v0 + 1), t1); lds_st<float>(ca(v0 + 2), t2);
            }
            ABAR();
            WAIT_L(0); ABAR();
            ABAR();
            STAMP(2);
            const u32 hd = WS_H + (u32)((j * 8 + mb) * 9 + 3 * nb) * 32768u + ax * 16384u + l16;
#pragma unroll 1
            for (int t = 0; t < 3; ++t) {
                u32x4 v[16];
                const u32 a = t * 32768u + ax * 16384u + l16;
#pragma unroll
                for (int i = 0; i < 16; ++i) DSR128(v[i], a, 1024 * i);
                asm volatile("s_waitcnt lgkmcnt(0)" : "+v"(v[0]), "+v"(v[1]), "+v"(v[2]), "+v"(v[3]), "+v"(v[4]), "+v"(v[5]), "+v"(v[6]), "+v"(v[7]),
                             "+v"(v[8]), "+v"(v[9]), "+v"(v[10]), "+v"(v[11]), "+v"(v[12]), "+v"(v[13]), "+v"(v[14]), "+v"(v[15]) :: "memory");
#pragma unroll
                for (int i = 0; i < 16; ++i) __builtin_amdgcn_raw_buffer_store_b128(v[i], rs_ws, hd + t * 32768u + i * 1024u, 0, 16);
                ABAR();
            }
            ABAR();
            WAIT_V(0);
            if (l == 0) st_flag(ws + WS_FH + ((j * 8 + mb) * 8 + 2 * nb + ax) * 4, tag);
            STAMP(3);
#pragma unroll 1
            for (int t = 4; t < 17; ++t) ABAR();
        } else {
            const int cn = mb & 3, mat = j >> 2;
            const int e0 = 144 * cn + 9 * (4 * (j & 3) + 2 * (mb >> 2) + ax);
            Row dr[3];
            {
                const rsrc_t rs_d = mk_rsrc(mat ? Di : Dr, D_ * D_ * 2);
#pragma unroll
                for (int p = 0; p < 3; ++p) dr[p] = rowload(rs_d, (u32)(e0 + (p < 2 ? 4 * p + q : 8)) * 1152u);
            }
            stats();
            ABAR();
#pragma unroll
            for (int p = 0; p < 3; ++p) {
                const float sm = rowsum(dr[p]);
                if (r16 == 0) __builtin_amdgcn_raw_buffer_store_b64((u32x2){__float_as_uint(sm), tag}, rs_ws, WS_DS + (u32)(576 * mat + e0 + (p < 2 ? 4 * p + q : 8)) * 8u, 0, 16);
                ABAR();
            }
            sg_load();
            ABAR();
            sg_done();
            ABAR();
            u32x2 dg[2];
            const int rho0 = ax ? 128 + r16 : l, rho1 = ax ? 128 + r16 : 64 + l;
            auto dl = [&]() {
                dg[0] = __builtin_amdgcn_raw_buffer_load_b64(rs_ws, WS_DS + (u32)(576 * mat + 144 * cn + colmap(cn, rho0 >> 4) + r16) * 8u, 0, 16);
                dg[1] = __builtin_amdgcn_raw_buffer_load_b64(rs_ws, WS_DS + (u32)(576 * mat + 144 * cn + colmap(cn, rho1 >> 4) + r16) * 8u, 0, 16);
            };
            dl();
            ABAR();
#pragma unroll 1
            for (int it = 0; it < SPIN_MAX; ++it) {
                if (__all(dg[0][1] == tag && dg[1][1] == tag)) break;
                __builtin_amdgcn_s_sleep(1);
                dl();
            }
            lds_st<u32>(L_CS + rho0 * 4, dg[0][0]); lds_st<u32>(L_CS + rho1 * 4, dg[1][0]);
            ABAR();
            WAIT_L(0); ABAR();
            if (bid == 255 && ax == 1) {
#pragma unroll 1
                for (int c = 0; c < 8; ++c) poll(ws + WS_STAT + (size_t)(c * 64 + l) * 64 + 4, tag);
                if (l == 0) st_flag(ws + WS_EPOCH, tag);
            }
            ABAR();
            STAMP(2);
#pragma unroll 1
            for (int t = 0; t < 17; ++t) ABAR();
        }
        ABAR();
    }

    if (w >= 8 || w == j) {
        const int owm = j & 3, own = j >> 2;
        const int b0 = (w >= 8) ? w - 8 : 8, nblk = (w >= 8) ? 2 : 1;
        poll(ws + WS_FP + ((g * 8 + j) * 8 + ((j + 1 + (l % 7)) & 7)) * 4, tag);
        STAMP(6);
#pragma unroll 1
        for (int bi = 0; bi < nblk; ++bi) {
            const int b = b0 + 4 * bi;
            u32x4 p[7];
#pragma unroll
            for (int k = 0; k < 7; ++k)
                p[k] = __builtin_amdgcn_raw_buffer_load_b128(rs_ws, l16, WS_P + (u32)(((g * 8 + j) * 9 + b) * 8 + ((j + 1 + k) & 7)) * 1024u, 16);
            const int P = (b < 8) ? (b >> 2) : 0, nf = (b < 8) ? 4 * own + (b & 3) : 8;
            const int trow = 256 * mb + 64 * owm + 32 * ((P + own) & 1) + r16, col = 144 * nb + colmap(nb, nf) + 4 * q;
            u32x2 x0 = *(const u32x2*)(x + (size_t)trow * D_ + col), x1 = *(const u32x2*)(x + (size_t)(trow + 16) * D_ + col);
            u32x4 o0 = lds_ld<u32x4>(b * 1024u + l16);
            u32x4 xx = {x0[0], x0[1], x1[0], x1[1]};
            u32 rr[4];
#pragma unroll
            for (int d = 0; d < 4; ++d) {
                float lo = bflo(o0[d]) + bflo(xx[d]), hi = bfhi(o0[d]) + bfhi(xx[d]);
#pragma unroll
                for (int k = 0; k < 7; ++k) { lo += bflo(p[k][d]); hi += bfhi(p[k][d]); }
                rr[d] = pk2(lo, hi);
            }
            *(u32x2*)(out + (size_t)trow * D_ + col) = (u32x2){rr[0], rr[1]};
            *(u32x2*)(out + (size_t)(trow + 16) * D_ + col) = (u32x2){rr[2], rr[3]};
        }
    }
}

extern "C" void kernel_launch(void* const* d_in, const int* in_sizes, int n_in, void* d_out, int out_size, void* d_ws, size_t ws_size,
                              hipStream_t stream) {
    (void)hipFuncSetAttribute((const void*)fused_kernel, hipFuncAttributeMaxDynamicSharedMemorySize, LDS_BYTES);
    hipLaunchKernelGGL(fused_kernel, dim3(256), dim3(768), LDS_BYTES, stream, (const u16*)d_in[0], (const u16*)d_in[1], (const u16*)d_in[2],
                       (const u16*)d_in[3], (const u16*)d_in[4], (const u16*)d_in[6], (const u16*)d_in[7], (u16*)d_out, (char*)d_ws);
}
```

```cpp
#define FAST0
#define KA
#define LNA 0
#define W2X
#define W2S 1
#define W2N 5
#define PC 12,20,24,28,32,36,40,44,48,48,48
#define EARLYF
#define NEWE
#define WL8 20
#define W5
#define EW 2
#define LW0 12
#define LW1 18
#define LW2 30
#define LW3 30
#define DH 0
#define DHD 0
#define CS2 9
#define XW 0
#define XS 4
#define XN 3
#define CODEB 34304u
#include <hip/hip_runtime.h>
#include <type_traits>
typedef unsigned short u16;
typedef unsigned u32;
typedef short bf16x8 __attribute__((ext_vector_type(8)));
typedef short s16x4 __attribute__((ext_vector_type(4)));
typedef short s16x2 __attribute__((ext_vector_type(2)));
typedef __bf16 bf16x2 __attribute__((ext_vector_type(2)));
typedef float f32x4 __attribute__((ext_vector_type(4)));
typedef float f32x2 __attribute__((ext_vector_type(2)));
typedef unsigned u32x4 __attribute__((ext_vector_type(4)));
typedef unsigned u32x2 __attribute__((ext_vector_type(2)));
#define LAS __attribute__((address_space(3)))
#define DEVI __device__ __forceinline__

constexpr int S_ = 2048, D_ = 576, F_ = 4608;
constexpr u32 WS_EPOCH = 0;
#define FS 128
#define HS 4
#define FW 1
#define PW_ 1
#define PS 4
constexpr u32 WS_FAB = 0x70000;
constexpr u32 WS_FH = 0x80000;
constexpr u32 WS_FP = 0xc0000;
constexpr u32 WS_STAT = 32768;
constexpr u32 WS_DS = 65536;
constexpr u32 WS_CP = 131072;
constexpr u32 WS_AB = 1u << 20;
constexpr u32 WS_H = 8u << 20;
constexpr u32 WS_P = 32u << 20;
constexpr u32 WS_END = 52u << 20;
constexpr u32 L_B = 98304;
constexpr u32 L_STAT = 159744;
constexpr u32 L_CS = 161792;
constexpr u32 L_TAG = 163328;
constexpr u32 LDS_BYTES = 163840;
constexpr int SPIN_MAX = 1 << 20;
#define WA1 0
#define WA2 0
#define WA3 0
#define WB 86, 86, 86, 86, 86, 86, 86, 86, 86
#define NB2 16
#define HAUX 16
#define PRE 10
#define PREG 10
#define ASKM (bit < 16u ? 0xffffull : ~0ull)
#define WN(x) (x)
#define PL 2
#define PRB 1
#define XM 7
#define XA 0
#define XB 16
#define XB2 1024u
#define XH 16
#define XH2 1024u
#define XC 32768u
#define XP 0
#define AS 8
#define WAS 2
#define EWN 6
#define FSL 4
#define OST 2
#define OAUX 0
#define CODEKB 32
#define WNT
#define NTC 0
#define NT1 0
#define NT2 0
#define NTX 0
#define LDPRIO 3
#define AXPRIO 3

extern __shared__ __attribute__((aligned(16))) char smem[];

#define WAIT_V(n) asm volatile("s_waitcnt vmcnt(" #n ")" ::: "memory")
#define WAIT_L(n) asm volatile("s_waitcnt lgkmcnt(" #n ")" ::: "memory")
#define BAR() __builtin_amdgcn_s_barrier()
#define STAMP(i)

#define TRC()
#define TR2()
#define TR3()
#define ABAR() do { asm volatile("" ::: "memory"); SB(); TRC(); __builtin_amdgcn_s_barrier(); TRC(); SB(); asm volatile("" ::: "memory"); } while (0)
#define SB() __builtin_amdgcn_sched_barrier(0)
#define DSR128(dst, addr, off) asm volatile("ds_read_b128 %0, %1 offset:%2" : "=v"(dst) : "v"(addr), "i"(off) : "memory")
#define DSRTR(dst, addr, off) asm volatile("ds_read_b64_tr_b16 %0, %1 offset:%2" : "=v"(dst) : "v"(addr), "i"(off) : "memory")
#define MFMA(c, a, b) asm volatile("v_mfma_f32_16x16x32_bf16 %0, %1, %2, %0" : "+v"(c) : "v"(a), "v"(b))
#define PIN(v) asm volatile("" : "+v"(v))
#define HOLD(v) asm volatile("s_nop 1" :: "v"(v) : "memory")
#define CAT(lo, hi) __builtin_shufflevector(lo, hi, 0, 1, 2, 3, 4, 5, 6, 7)
#define UNI(v) ((u32)__builtin_amdgcn_readfirstlane(v))
typedef __amdgpu_buffer_rsrc_t rsrc_t;
DEVI rsrc_t mk_rsrc(const void* p, u32 bytes) { return __builtin_amdgcn_make_buffer_rsrc((void*)p, 0, bytes, 0x00020000); }
template <int AUX, int I> DEVI void dma(rsrc_t rs, u32 lds, u32 voff, u32 soff) {
    __builtin_amdgcn_raw_ptr_buffer_load_lds(rs, (LAS void*)((LAS char*)smem + lds + (I >> 2) * 4096u), 16, voff, soff - (I & 3) * 1024u, (I & 3) * 1024, AUX);
}
template <int N> constexpr std::integral_constant<int, N> IC{};
template <int N, typename F> DEVI void sfor(F f) {
    if constexpr (N > 0) { sfor<N - 1>(f); f(std::integral_constant<int, N - 1>{}); }
}
DEVI f32x4 mfma(bf16x8 a, bf16x8 b, f32x4 c) { return __builtin_amdgcn_mfma_f32_16x16x32_bf16(a, b, c, 0, 0, 0); }
DEVI u32 pk2(float a, float b) { bf16x2 v = {(__bf16)a, (__bf16)b}; return __builtin_bit_cast(u32, v); }
DEVI float bflo(u32 v) { return __uint_as_float(v << 16); }
DEVI float bfhi(u32 v) { return __uint_as_float(v & 0xffff0000u); }
DEVI float dot1(u32 v, float acc) {
    return __builtin_amdgcn_fdot2_f32_bf16(__builtin_bit_cast(bf16x2, v), __builtin_bit_cast(bf16x2, 0x3f803f80u), acc, false);
}
DEVI f32x2 add2(u32 v, f32x2 acc) { return acc + (f32x2){bflo(v), bfhi(v)}; }
template <int CTRL> DEVI float dpp_add(float v) {
    return v + __builtin_bit_cast(float, __builtin_amdgcn_update_dpp(0, __builtin_bit_cast(int, v), CTRL, 0xf, 0xf, true));
}
DEVI float ror8(float v) { return __builtin_bit_cast(float, __builtin_amdgcn_update_dpp(0, __builtin_bit_cast(int, v), 0x128, 0xf, 0xf, true)); }
DEVI float row16_sum(float v) { v = dpp_add<0xB1>(v); v = dpp_add<0x4E>(v); v = dpp_add<0x141>(v); return dpp_add<0x140>(v); }
DEVI float wave_sum(float v) {
    v = row16_sum(v);
    auto a = __builtin_amdgcn_permlane16_swap(__float_as_uint(v), __float_as_uint(v), false, false);
    v = __uint_as_float(a[0]) + __uint_as_float(a[1]);
    auto b = __builtin_amdgcn_permlane32_swap(__float_as_uint(v), __float_as_uint(v), false, false);
    return __uint_as_float(b[0]) + __uint_as_float(b[1]);
}
DEVI u32 ld_flag(const char* p) { return __hip_atomic_load((const u32*)p, __ATOMIC_RELAXED, __HIP_MEMORY_SCOPE_AGENT); }
DEVI void st_flag(char* p, u32 v) { __hip_atomic_store((u32*)p, v, __ATOMIC_RELAXED, __HIP_MEMORY_SCOPE_AGENT); }
DEVI void poll(const char* p, u32 tag) {
#pragma unroll 1
    for (int it = 0; it < SPIN_MAX; ++it) {
        u32 f = ld_flag(p);
        if (__all(f == tag)) break;
        __builtin_amdgcn_s_sleep(1);
    }
}
template <typename T> DEVI T lds_ld(u32 a) { return *(LAS T*)((LAS char*)smem + a); }
template <typename T> DEVI void lds_st(u32 a, T v) { *(LAS T*)((LAS char*)smem + a) = v; }
DEVI u32 colmap(int n, int nf) {
    const u32 pc = (n == 1 || n == 2) ? 0x3021u : 0x3210u, o = n & 1;
    return nf < 8 ? 16u * o + 32u * ((pc >> (4 * (nf >> 1))) & 3u) + 16u * (nf & 1) : (o ? 0u : 128u);
}

__global__ __launch_bounds__(768) void fused_kernel(const u16* __restrict__ x, const u16* __restrict__ Cr, const u16* __restrict__ Ci,
                                                    const u16* __restrict__ Dr, const u16* __restrict__ Di, const u16* __restrict__ w1,
                                                    const u16* __restrict__ w2, u16* __restrict__ out, char* __restrict__ ws) {
    unsigned long long xp_;
    u32 kt_;
    asm volatile("s_load_dwordx2 %0, %2, 0\n\ts_load_dword %1, %2, 0x40" : "=&s"(xp_), "=&s"(kt_) : "s"(__builtin_amdgcn_kernarg_segment_ptr()) : "memory");
    SB();
    const int tid = threadIdx.x, l = tid & 63, w = __builtin_amdgcn_readfirstlane(tid >> 6);
    const int bid = blockIdx.x, j = bid & 7, g = bid >> 3, mb = g >> 2, nb = g & 3;
    STAMP(0);
    TR3();
    if (w >= 10) {
        unsigned long long pc_;
        asm volatile("s_getpc_b64 %0" : "=s"(pc_));
        asm volatile("global_load_dwordx4 v[152:155], %0, %1" :: "v"(min((u32)(tid * 16 + g * 1024 + w * 31744 - 327680), CODEB - 16u)), "s"(pc_) : "memory", "v152", "v153", "v154", "v155");
    }
    SB();
    if ((w >> 1) == 4) {
        const u32 ab = (u32)(nb + 1) >> 2, r = (u32)l >> 3;
        const u32 v0 = (r + (r & (4u * ab))) * 1152u + 16u * ((l & 7) ^ (l >> 4)), v1 = (v0 ^ 64u) + (8u * 1152u - 1024u) + ab * (8u * 1152u);
        const u32 rt = 2u * (u32)(mb + ab * (2 * (j & 3) + (mb >> 2) - mb)) + (w & 1);
        const u32 s0 = rt * (128u * 1152u), s1 = s0 + 16u * 1152u - ab * (12u * 1152u), lb = (w & 1) * 16384u;
        asm volatile("s_waitcnt lgkmcnt(0)" : "+s"(xp_) : "s"(s1), "v"(v1) : "memory");
        const rsrc_t rs_x = mk_rsrc((const void*)xp_, S_ * D_ * 2);
        sfor<16>([&](auto i) { dma<NTX, i>(rs_x, lb, (i & 1) ? v1 : v0, ((i & 2) ? s1 : s0) + (u32)(i >> 2) * (32u * 1152u) + (i & 1) * 1024u); });
        SB();
    }
    asm volatile("s_waitcnt lgkmcnt(0)" :: "s"(xp_), "s"(kt_) : "memory");
    SB();
    const int q = l >> 4, r16 = l & 15;
    const bool is_ab = nb == 3;
    const int row0 = is_ab ? 512 * (j & 3) + 256 * (mb >> 2) : 256 * mb;
    const int c0 = is_ab ? 0 : 3;
    const int cn = (nb + 1 + mb) & 3;
    const rsrc_t rs_ws = mk_rsrc(ws, WS_END);
    const u32 l16 = l * 16;
    u32 lv = l16;
    u32 tag;
    if (0) {
        unsigned long long pc_;
        asm volatile("s_getpc_b64 %0" : "=s"(pc_));
        asm volatile("global_load_dwordx4 v[152:155], %0, %1" :: "v"(l16), "s"((pc_ & ~127ull) + (g + 32 * (w - 10)) * 1024) : "memory", "v152", "v153", "v154", "v155");
    }
    asm volatile("" :: "s"(x), "s"(Cr), "s"(Ci), "s"(Dr), "s"(Di), "s"(w1), "s"(w2), "s"(out), "s"(ws));

    if (w < 8) {
        const int wm = w & 3, wn = w >> 2;
        WAIT_V(0);
        const u32 trl = (u32)((l >> 2) & 7) * 64u + 32u * (q & 1) + 8u * (l & 3);
        const u32 rowl = (u32)r16 * 128u + 16u * (q ^ (r16 >> 1));
        if (!is_ab) {
            u32 rA = (u32)wm * 8192u + rowl;
            u32 rB = L_B + (u32)(l >> 5) * 6144u + (u32)wn * 1536u + trl;
            u32 a, b0, b1, sA = 0, sB = 0, sN = 8;
            const u32 sH0 = (u32)wm * 8192u + (u32)wn * 33792u, sH1 = (u32)wm * 8192u + (wn ? 65536u : 1024u), sSt = L_STAT + (u32)wm * 256u, sCs = L_CS + (u32)wn * 384u;
            asm volatile(
                "v_mov_b32 v16, 0\n\t"
                "v_mov_b32 v17, 0\n\t"
                "v_mov_b32 v18, 0\n\t"
                "v_mov_b32 v19, 0\n\t"
                "v_mov_b32 v20, 0\n\t"
                "v_mov_b32 v21, 0\n\t"
                "v_mov_b32 v22, 0\n\t"
                "v_mov_b32 v23, 0\n\t"
                "v_mov_b32 v24, 0\n\t"
                "v_mov_b32 v25, 0\n\t"
                "v_mov_b32 v26, 0\n\t"
                "v_mov_b32 v27, 0\n\t"
                "v_mov_b32 v28, 0\n\t"
                "v_mov_b32 v29, 0\n\t"
                "v_mov_b32 v30, 0\n\t"
                "v_mov_b32 v31, 0\n\t"
                "v_mov_b32 v32, 0\n\t"
                "v_mov_b32 v33, 0\n\t"
                "v_mov_b32 v34, 0\n\t"
                "v_mov_b32 v35, 0\n\t"
                "v_mov_b32 v36, 0\n\t"
                "v_mov_b32 v37, 0\n\t"
                "v_mov_b32 v38, 0\n\t"
                "v_mov_b32 v39, 0\n\t"
                "v_mov_b32 v40, 0\n\t"
                "v_mov_b32 v41, 0\n\t"
                "v_mov_b32 v42, 0\n\t"
                "v_mov_b32 v43, 0\n\t"
                "v_mov_b32 v44, 0\n\t"
                "v_mov_b32 v45, 0\n\t"
                "v_mov_b32 v46, 0\n\t"
                "v_mov_b32 v47, 0\n\t"
                "v_mov_b32 v48, 0\n\t"
                "v_mov_b32 v49, 0\n\t"
                "v_mov_b32 v50, 0\n\t"
                "v_mov_b32 v51, 0\n\t"
                "v_mov_b32 v52, 0\n\t"
                "v_mov_b32 v53, 0\n\t"
                "v_mov_b32 v54, 0\n\t"
                "v_mov_b32 v55, 0\n\t"
                "v_mov_b32 v56, 0\n\t"
                "v_mov_b32 v57, 0\n\t"
                "v_mov_b32 v58, 0\n\t"
                "v_mov_b32 v59, 0\n\t"
                "v_mov_b32 v60, 0\n\t"
                "v_mov_b32 v61, 0\n\t"
                "v_mov_b32 v62, 0\n\t"
                "v_mov_b32 v63, 0\n\t"
                "v_mov_b32 v64, 0\n\t"
                "v_mov_b32 v65, 0\n\t"
                "v_mov_b32 v66, 0\n\t"
                "v_mov_b32 v67, 0\n\t"
                "v_mov_b32 v68, 0\n\t"
                "v_mov_b32 v69, 0\n\t"
                "v_mov_b32 v70, 0\n\t"
                "v_mov_b32 v71, 0\n\t"
                "v_mov_b32 v72, 0\n\t"
                "v_mov_b32 v73, 0\n\t"
                "v_mov_b32 v74, 0\n\t"
                "v_mov_b32 v75, 0\n\t"
                "v_mov_b32 v76, 0\n\t"
                "v_mov_b32 v77, 0\n\t"
                "v_mov_b32 v78, 0\n\t"
                "v_mov_b32 v79, 0\n\t"
                "v_mov_b32 v80, 0\n\t"
                "v_mov_b32 v81, 0\n\t"
                "v_mov_b32 v82, 0\n\t"
                "v_mov_b32 v83, 0\n\t"
                "v_mov_b32 v84, 0\n\t"
                "v_mov_b32 v85, 0\n\t"
                "v_mov_b32 v86, 0\n\t"
                "v_mov_b32 v87, 0\n\t"
                "v_mov_b32 v88, 0\n\t"
                "v_mov_b32 v89, 0\n\t"
                "v_mov_b32 v90, 0\n\t"
                "v_mov_b32 v91, 0\n\t"
                "v_mov_b32 v92, 0\n\t"
                "v_mov_b32 v93, 0\n\t"
                "v_mov_b32 v94, 0\n\t"
                "v_mov_b32 v95, 0\n\t"
                "v_mov_b32 v96, 0\n\t"
                "v_mov_b32 v97, 0\n\t"
                "v_mov_b32 v98, 0\n\t"
                "v_mov_b32 v99, 0\n\t"
                "v_mov_b32 v100, 0\n\t"
                "v_mov_b32 v101, 0\n\t"
                "v_mov_b32 v102, 0\n\t"
                "v_mov_b32 v103, 0\n\t"
                "v_mov_b32 v104, 0\n\t"
                "v_mov_b32 v105, 0\n\t"
                "v_mov_b32 v106, 0\n\t"
                "v_mov_b32 v107, 0\n\t"
                "v_mov_b32 v108, 0\n\t"
                "v_mov_b32 v109, 0\n\t"
                "v_mov_b32 v110, 0\n\t"
                "v_mov_b32 v111, 0\n\t"
                "v_mov_b32 %[a], %[rA]\n\t"
                "v_add_u32 %[b0], %[sB], %[rB]\n\t"
                "v_xor_b32 %[b1], 32, %[b0]\n\t"
                "s_barrier\n\t"
                "ds_read_b64_tr_b16 v[144:145], %[b0] offset:0\n\t"
                "ds_read_b64_tr_b16 v[146:147], %[b0] offset:3072\n\t"
                "ds_read_b128 v[112:115], %[a] offset:0\n\t"
                "ds_read_b128 v[116:119], %[a] offset:2048\n\t"
                "ds_read_b128 v[120:123], %[a] offset:4096\n\t"
                "ds_read_b128 v[124:127], %[a] offset:6144\n\t"
                "ds_read_b64_tr_b16 v[148:149], %[b1] offset:0\n\t"
                "ds_read_b64_tr_b16 v[150:151], %[b1] offset:3072\n\t"
                "ds_read_b64_tr_b16 v[152:153], %[b0] offset:512\n\t"
                "ds_read_b64_tr_b16 v[154:155], %[b0] offset:3584\n\t"
                "ds_read_b64_tr_b16 v[156:157], %[b1] offset:512\n\t"
                "ds_read_b64_tr_b16 v[158:159], %[b1] offset:3584\n\t"
                "ds_read_b64_tr_b16 v[160:161], %[b0] offset:1024\n\t"
                "ds_read_b64_tr_b16 v[162:163], %[b0] offset:4096\n\t"
                "ds_read_b64_tr_b16 v[164:165], %[b1] offset:1024\n\t"
                "ds_read_b64_tr_b16 v[166:167], %[b1] offset:4096\n\t"
                "s_add_u32 %[sB], %[sB], 12288\n\t"
                "s_cmp_lt_u32 %[sB], 61440\n\t"
                "s_cselect_b32 %[sB], %[sB], 0\n\t"
                ".p2align 6\n\t"
                "L1%=:\n\t"
                "s_waitcnt lgkmcnt(10)\n\t"
                "v_mfma_f32_16x16x32_bf16 v[16:19], v[144:147], v[112:115], v[16:19]\n\t"
                "v_add_u32 %[b0], %[sB], %[rB]\n\t"
                "v_xor_b32 %[b1], 32, %[b0]\n\t"
                "v_mfma_f32_16x16x32_bf16 v[40:43], v[144:147], v[116:119], v[40:43]\n\t"
                "v_xor_b32 %[a], 64, %[a]\n\t"
                "v_mfma_f32_16x16x32_bf16 v[64:67], v[144:147], v[120:123], v[64:67]\n\t"
                "v_mfma_f32_16x16x32_bf16 v[88:91], v[144:147], v[124:127], v[88:91]\n\t"
                "ds_read_b64_tr_b16 v[144:145], %[b0] offset:0\n\t"
                "ds_read_b64_tr_b16 v[146:147], %[b0] offset:3072\n\t"
                "s_waitcnt lgkmcnt(10)\n\t"
                "v_mfma_f32_16x16x32_bf16 v[20:23], v[148:151], v[112:115], v[20:23]\n\t"
                "ds_read_b128 v[128:131], %[a] offset:0\n\t"
                "v_mfma_f32_16x16x32_bf16 v[44:47], v[148:151], v[116:119], v[44:47]\n\t"
                "ds_read_b128 v[132:135], %[a] offset:2048\n\t"
                "v_mfma_f32_16x16x32_bf16 v[68:71], v[148:151], v[120:123], v[68:71]\n\t"
                "ds_read_b128 v[136:139], %[a] offset:4096\n\t"
                "v_mfma_f32_16x16x32_bf16 v[92:95], v[148:151], v[124:127], v[92:95]\n\t"
                "ds_read_b128 v[140:143], %[a] offset:6144\n\t"
                "ds_read_b64_tr_b16 v[148:149], %[b1] offset:0\n\t"
                "ds_read_b64_tr_b16 v[150:151], %[b1] offset:3072\n\t"
                "s_waitcnt lgkmcnt(14)\n\t"
                "v_mfma_f32_16x16x32_bf16 v[24:27], v[152:155], v[112:115], v[24:27]\n\t"
                "v_mfma_f32_16x16x32_bf16 v[48:51], v[152:155], v[116:119], v[48:51]\n\t"
                "v_mfma_f32_16x16x32_bf16 v[72:75], v[152:155], v[120:123], v[72:75]\n\t"
                "v_mfma_f32_16x16x32_bf16 v[96:99], v[152:155], v[124:127], v[96:99]\n\t"
                "ds_read_b64_tr_b16 v[152:153], %[b0] offset:512\n\t"
                "ds_read_b64_tr_b16 v[154:155], %[b0] offset:3584\n\t"
                "s_waitcnt lgkmcnt(14)\n\t"
                "v_mfma_f32_16x16x32_bf16 v[28:31], v[156:159], v[112:115], v[28:31]\n\t"
                "s_add_u32 %[sB], %[sB], 12288\n\t"
                "s_cmp_lt_u32 %[sB], 61440\n\t"
                "s_cselect_b32 %[sB], %[sB], 0\n\t"
                "v_mfma_f32_16x16x32_bf16 v[52:55], v[156:159], v[116:119], v[52:55]\n\t"
                "s_add_u32 %[sA], %[sA], 0x8000\n\t"
                "s_cmp_lt_u32 %[sA], 0x18000\n\t"
                "s_cselect_b32 %[sA], %[sA], 0\n\t"
                "v_mfma_f32_16x16x32_bf16 v[76:79], v[156:159], v[120:123], v[76:79]\n\t"
                "v_mfma_f32_16x16x32_bf16 v[100:103], v[156:159], v[124:127], v[100:103]\n\t"
                "ds_read_b64_tr_b16 v[156:157], %[b1] offset:512\n\t"
                "ds_read_b64_tr_b16 v[158:159], %[b1] offset:3584\n\t"
                "s_waitcnt lgkmcnt(14)\n\t"
                "v_mfma_f32_16x16x32_bf16 v[32:35], v[160:163], v[112:115], v[32:35]\n\t"
                "v_mfma_f32_16x16x32_bf16 v[56:59], v[160:163], v[116:119], v[56:59]\n\t"
                "v_mfma_f32_16x16x32_bf16 v[80:83], v[160:163], v[120:123], v[80:83]\n\t"
                "v_mfma_f32_16x16x32_bf16 v[104:107], v[160:163], v[124:127], v[104:107]\n\t"
                "ds_read_b64_tr_b16 v[160:161], %[b0] offset:1024\n\t"
                "ds_read_b64_tr_b16 v[162:163], %[b0] offset:4096\n\t"
                "s_waitcnt lgkmcnt(14)\n\t"
                "v_mfma_f32_16x16x32_bf16 v[36:39], v[164:167], v[112:115], v[36:39]\n\t"
                "v_mfma_f32_16x16x32_bf16 v[60:63], v[164:167], v[116:119], v[60:63]\n\t"
                "v_mfma_f32_16x16x32_bf16 v[84:87], v[164:167], v[120:123], v[84:87]\n\t"
                "v_mfma_f32_16x16x32_bf16 v[108:111], v[164:167], v[124:127], v[108:111]\n\t"
                "ds_read_b64_tr_b16 v[164:165], %[b1] offset:1024\n\t"
                "ds_read_b64_tr_b16 v[166:167], %[b1] offset:4096\n\t"
                "s_waitcnt lgkmcnt(10)\n\t"
                "v_mfma_f32_16x16x32_bf16 v[16:19], v[144:147], v[128:131], v[16:19]\n\t"
                "v_add_u32 %[b0], %[sB], %[rB]\n\t"
                "v_xor_b32 %[b1], 32, %[b0]\n\t"
                "v_mfma_f32_16x16x32_bf16 v[40:43], v[144:147], v[132:135], v[40:43]\n\t"
                "v_add_u32 %[a], %[sA], %[rA]\n\t"
                "v_mfma_f32_16x16x32_bf16 v[64:67], v[144:147], v[136:139], v[64:67]\n\t"
                "v_mfma_f32_16x16x32_bf16 v[88:91], v[144:147], v[140:143], v[88:91]\n\t"
                "s_waitcnt lgkmcnt(0)\n\t"
                "s_barrier\n\t"
                "ds_read_b64_tr_b16 v[144:145], %[b0] offset:0\n\t"
                "ds_read_b64_tr_b16 v[146:147], %[b0] offset:3072\n\t"
                "v_mfma_f32_16x16x32_bf16 v[20:23], v[148:151], v[128:131], v[20:23]\n\t"
                "ds_read_b128 v[112:115], %[a] offset:0\n\t"
                "v_mfma_f32_16x16x32_bf16 v[44:47], v[148:151], v[132:135], v[44:47]\n\t"
                "ds_read_b128 v[116:119], %[a] offset:2048\n\t"
                "v_mfma_f32_16x16x32_bf16 v[68:71], v[148:151], v[136:139], v[68:71]\n\t"
                "ds_read_b128 v[120:123], %[a] offset:4096\n\t"
                "v_mfma_f32_16x16x32_bf16 v[92:95], v[148:151], v[140:143], v[92:95]\n\t"
                "ds_read_b128 v[124:127], %[a] offset:6144\n\t"
                "ds_read_b64_tr_b16 v[148:149], %[b1] offset:0\n\t"
                "ds_read_b64_tr_b16 v[150:151], %[b1] offset:3072\n\t"
                "v_mfma_f32_16x16x32_bf16 v[24:27], v[152:155], v[128:131], v[24:27]\n\t"
                "v_mfma_f32_16x16x32_bf16 v[48:51], v[152:155], v[132:135], v[48:51]\n\t"
                "v_mfma_f32_16x16x32_bf16 v[72:75], v[152:155], v[136:139], v[72:75]\n\t"
                "v_mfma_f32_16x16x32_bf16 v[96:99], v[152:155], v[140:143], v[96:99]\n\t"
                "ds_read_b64_tr_b16 v[152:153], %[b0] offset:512\n\t"
                "ds_read_b64_tr_b16 v[154:155], %[b0] offset:3584\n\t"
                "v_mfma_f32_16x16x32_bf16 v[28:31], v[156:159], v[128:131], v[28:31]\n\t"
                "s_add_u32 %[sB], %[sB], 12288\n\t"
                "s_cmp_lt_u32 %[sB], 61440\n\t"
                "s_cselect_b32 %[sB], %[sB], 0\n\t"
                "v_mfma_f32_16x16x32_bf16 v[52:55], v[156:159], v[132:135], v[52:55]\n\t"
                "v_mfma_f32_16x16x32_bf16 v[76:79], v[156:159], v[136:139], v[76:79]\n\t"
                "s_sub_u32 %[sN], %[sN], 1\n\t"
                "s_cmp_eq_u32 %[sN], 0\n\t"
                "v_mfma_f32_16x16x32_bf16 v[100:103], v[156:159], v[140:143], v[100:103]\n\t"
                "ds_read_b64_tr_b16 v[156:157], %[b1] offset:512\n\t"
                "ds_read_b64_tr_b16 v[158:159], %[b1] offset:3584\n\t"
                "v_mfma_f32_16x16x32_bf16 v[32:35], v[160:163], v[128:131], v[32:35]\n\t"
                "v_mfma_f32_16x16x32_bf16 v[56:59], v[160:163], v[132:135], v[56:59]\n\t"
                "v_mfma_f32_16x16x32_bf16 v[80:83], v[160:163], v[136:139], v[80:83]\n\t"
                "v_mfma_f32_16x16x32_bf16 v[104:107], v[160:163], v[140:143], v[104:107]\n\t"
                "ds_read_b64_tr_b16 v[160:161], %[b0] offset:1024\n\t"
                "ds_read_b64_tr_b16 v[162:163], %[b0] offset:4096\n\t"
                "v_mfma_f32_16x16x32_bf16 v[36:39], v[164:167], v[128:131], v[36:39]\n\t"
                "v_mfma_f32_16x16x32_bf16 v[60:63], v[164:167], v[132:135], v[60:63]\n\t"
                "v_mfma_f32_16x16x32_bf16 v[84:87], v[164:167], v[136:139], v[84:87]\n\t"
                "v_mfma_f32_16x16x32_bf16 v[108:111], v[164:167], v[140:143], v[108:111]\n\t"
                "ds_read_b64_tr_b16 v[164:165], %[b1] offset:1024\n\t"
                "ds_read_b64_tr_b16 v[166:167], %[b1] offset:4096\n\t"
                "s_cbranch_scc0 L1%=\n\t"
                "s_waitcnt lgkmcnt(13)\n\t"
                "v_mfma_f32_16x16x32_bf16 v[16:19], v[144:147], v[112:115], v[16:19]\n\t"
                "v_and_b32 v12, 0xf0, %[l16]\n\t"
                "v_xor_b32 %[a], 64, %[a]\n\t"
                "v_add_u32 v12, %[sSt], v12\n\t"
                "v_lshrrev_b32 %[rA], 4, %[l16]\n\t"
                "v_add_u32 %[b0], %[sB], %[rB]\n\t"
                "v_xor_b32 %[b1], 32, %[b0]\n\t"
                "ds_read_b128 v[8:11], v12\n\t"
                "ds_read_b128 v[12:15], v12 offset:1024\n\t"
                "s_waitcnt lgkmcnt(14)\n\t"
                "v_mfma_f32_16x16x32_bf16 v[40:43], v[144:147], v[116:119], v[40:43]\n\t"
                "v_and_b32 %[rB], 0xc0, %[l16]\n\t"
                "v_and_or_b32 %[rB], %[rA], 48, %[rB]\n\t"
                "v_and_b32 %[rA], 3, %[rA]\n\t"
                "v_lshl_add_u32 %[rA], %[rA], 2, %[rB]\n\t"
                "v_add_u32 %[rA], %[sCs], %[rA]\n\t"
                "ds_read_b32 %[rB], %[rA] offset:256\n\t"
                "ds_read_b32 %[rA], %[rA]\n\t"
                "s_waitcnt lgkmcnt(15)\n\t"
                "v_mfma_f32_16x16x32_bf16 v[64:67], v[144:147], v[120:123], v[64:67]\n\t"
                "ds_read_b128 v[128:131], %[a] offset:0\n\t"
                "ds_read_b128 v[132:135], %[a] offset:2048\n\t"
                "s_waitcnt lgkmcnt(15)\n\t"
                "v_mfma_f32_16x16x32_bf16 v[88:91], v[144:147], v[124:127], v[88:91]\n\t"
                "ds_read_b128 v[136:139], %[a] offset:4096\n\t"
                "ds_read_b128 v[140:143], %[a] offset:6144\n\t"
                "ds_read_b64_tr_b16 v[144:145], %[b0] offset:0\n\t"
                "ds_read_b64_tr_b16 v[146:147], %[b0] offset:3072\n\t"
                "s_waitcnt lgkmcnt(6)\n\t"
                "v_fmac_f32_dpp v16, %[rA], v8 row_newbcast:0 row_mask:0xf bank_mask:0xf\n\t"
                "v_fmac_f32_dpp v17, %[rA], v8 row_newbcast:1 row_mask:0xf bank_mask:0xf\n\t"
                "v_fmac_f32_dpp v18, %[rA], v8 row_newbcast:2 row_mask:0xf bank_mask:0xf\n\t"
                "v_fmac_f32_dpp v19, %[rA], v8 row_newbcast:3 row_mask:0xf bank_mask:0xf\n\t"
                "v_fmac_f32_dpp v40, %[rA], v9 row_newbcast:0 row_mask:0xf bank_mask:0xf\n\t"
                "v_mfma_f32_16x16x32_bf16 v[20:23], v[148:151], v[112:115], v[20:23]\n\t"
                "v_add_u32 %[a], %[sH0], %[l16]\n\t"
                "v_fmac_f32_dpp v41, %[rA], v9 row_newbcast:1 row_mask:0xf bank_mask:0xf\n\t"
                "v_fmac_f32_dpp v42, %[rA], v9 row_newbcast:2 row_mask:0xf bank_mask:0xf\n\t"
                "v_fmac_f32_dpp v43, %[rA], v9 row_newbcast:3 row_mask:0xf bank_mask:0xf\n\t"
                "v_fmac_f32_dpp v64, %[rA], v10 row_newbcast:0 row_mask:0xf bank_mask:0xf\n\t"
                "v_fmac_f32_dpp v65, %[rA], v10 row_newbcast:1 row_mask:0xf bank_mask:0xf\n\t"
                "v_mfma_f32_16x16x32_bf16 v[44:47], v[148:151], v[116:119], v[44:47]\n\t"
                "v_fmac_f32_dpp v66, %[rA], v10 row_newbcast:2 row_mask:0xf bank_mask:0xf\n\t"
                "v_fmac_f32_dpp v67, %[rA], v10 row_newbcast:3 row_mask:0xf bank_mask:0xf\n\t"
                "v_fmac_f32_dpp v88, %[rA], v11 row_newbcast:0 row_mask:0xf bank_mask:0xf\n\t"
                "v_fmac_f32_dpp v89, %[rA], v11 row_newbcast:1 row_mask:0xf bank_mask:0xf\n\t"
                "v_fmac_f32_dpp v90, %[rA], v11 row_newbcast:2 row_mask:0xf bank_mask:0xf\n\t"
                "v_mfma_f32_16x16x32_bf16 v[68:71], v[148:151], v[120:123], v[68:71]\n\t"
                "v_fmac_f32_dpp v91, %[rA], v11 row_newbcast:3 row_mask:0xf bank_mask:0xf\n\t"
                "v_fmac_f32_dpp v20, %[rA], v8 row_newbcast:4 row_mask:0xf bank_mask:0xf\n\t"
                "v_fmac_f32_dpp v21, %[rA], v8 row_newbcast:5 row_mask:0xf bank_mask:0xf\n\t"
                "v_fmac_f32_dpp v22, %[rA], v8 row_newbcast:6 row_mask:0xf bank_mask:0xf\n\t"
                "v_fmac_f32_dpp v23, %[rA], v8 row_newbcast:7 row_mask:0xf bank_mask:0xf\n\t"
                "v_mfma_f32_16x16x32_bf16 v[92:95], v[148:151], v[124:127], v[92:95]\n\t"
                "ds_read_b64_tr_b16 v[148:149], %[b1] offset:0\n\t"
                "ds_read_b64_tr_b16 v[150:151], %[b1] offset:3072\n\t"
                "v_fmac_f32_dpp v44, %[rA], v9 row_newbcast:4 row_mask:0xf bank_mask:0xf\n\t"
                "v_fmac_f32_dpp v45, %[rA], v9 row_newbcast:5 row_mask:0xf bank_mask:0xf\n\t"
                "v_fmac_f32_dpp v46, %[rA], v9 row_newbcast:6 row_mask:0xf bank_mask:0xf\n\t"
                "v_fmac_f32_dpp v47, %[rA], v9 row_newbcast:7 row_mask:0xf bank_mask:0xf\n\t"
                "v_fmac_f32_dpp v68, %[rA], v10 row_newbcast:4 row_mask:0xf bank_mask:0xf\n\t"
                "v_mfma_f32_16x16x32_bf16 v[24:27], v[152:155], v[112:115], v[24:27]\n\t"
                "v_fmac_f32_dpp v69, %[rA], v10 row_newbcast:5 row_mask:0xf bank_mask:0xf\n\t"
                "v_fmac_f32_dpp v70, %[rA], v10 row_newbcast:6 row_mask:0xf bank_mask:0xf\n\t"
                "v_fmac_f32_dpp v71, %[rA], v10 row_newbcast:7 row_mask:0xf bank_mask:0xf\n\t"
                "v_fmac_f32_dpp v92, %[rA], v11 row_newbcast:4 row_mask:0xf bank_mask:0xf\n\t"
                "v_fmac_f32_dpp v93, %[rA], v11 row_newbcast:5 row_mask:0xf bank_mask:0xf\n\t"
                "v_mfma_f32_16x16x32_bf16 v[48:51], v[152:155], v[116:119], v[48:51]\n\t"
                "v_fmac_f32_dpp v94, %[rA], v11 row_newbcast:6 row_mask:0xf bank_mask:0xf\n\t"
                "v_fmac_f32_dpp v95, %[rA], v11 row_newbcast:7 row_mask:0xf bank_mask:0xf\n\t"
                "v_fmac_f32_dpp v28, %[rA], v8 row_newbcast:12 row_mask:0xf bank_mask:0xf\n\t"
                "v_fmac_f32_dpp v29, %[rA], v8 row_newbcast:13 row_mask:0xf bank_mask:0xf\n\t"
                "v_fmac_f32_dpp v30, %[rA], v8 row_newbcast:14 row_mask:0xf bank_mask:0xf\n\t"
                "v_mfma_f32_16x16x32_bf16 v[72:75], v[152:155], v[120:123], v[72:75]\n\t"
                "v_fmac_f32_dpp v31, %[rA], v8 row_newbcast:15 row_mask:0xf bank_mask:0xf\n\t"
                "v_fmac_f32_dpp v52, %[rA], v9 row_newbcast:12 row_mask:0xf bank_mask:0xf\n\t"
                "v_fmac_f32_dpp v53, %[rA], v9 row_newbcast:13 row_mask:0xf bank_mask:0xf\n\t"
                "v_fmac_f32_dpp v54, %[rA], v9 row_newbcast:14 row_mask:0xf bank_mask:0xf\n\t"
                "v_fmac_f32_dpp v55, %[rA], v9 row_newbcast:15 row_mask:0xf bank_mask:0xf\n\t"
                "v_mfma_f32_16x16x32_bf16 v[96:99], v[152:155], v[124:127], v[96:99]\n\t"
                "ds_read_b64_tr_b16 v[152:153], %[b0] offset:512\n\t"
                "ds_read_b64_tr_b16 v[154:155], %[b0] offset:3584\n\t"
                "v_fmac_f32_dpp v76, %[rA], v10 row_newbcast:12 row_mask:0xf bank_mask:0xf\n\t"
                "v_fmac_f32_dpp v77, %[rA], v10 row_newbcast:13 row_mask:0xf bank_mask:0xf\n\t"
                "v_fmac_f32_dpp v78, %[rA], v10 row_newbcast:14 row_mask:0xf bank_mask:0xf\n\t"
                "v_fmac_f32_dpp v79, %[rA], v10 row_newbcast:15 row_mask:0xf bank_mask:0xf\n\t"
                "v_fmac_f32_dpp v100, %[rA], v11 row_newbcast:12 row_mask:0xf bank_mask:0xf\n\t"
                "s_waitcnt lgkmcnt(4)\n\t"
                "v_mfma_f32_16x16x32_bf16 v[16:19], v[144:147], v[128:131], v[16:19]\n\t"
                "v_fmac_f32_dpp v101, %[rA], v11 row_newbcast:13 row_mask:0xf bank_mask:0xf\n\t"
                "v_fmac_f32_dpp v102, %[rA], v11 row_newbcast:14 row_mask:0xf bank_mask:0xf\n\t"
                "v_fmac_f32_dpp v103, %[rA], v11 row_newbcast:15 row_mask:0xf bank_mask:0xf\n\t"
                "v_fmac_f32_dpp v24, %[rA], v8 row_newbcast:8 row_mask:0xf bank_mask:0xf\n\t"
                "v_fmac_f32_dpp v25, %[rA], v8 row_newbcast:9 row_mask:0xf bank_mask:0xf\n\t"
                "v_mfma_f32_16x16x32_bf16 v[40:43], v[144:147], v[132:135], v[40:43]\n\t"
                "v_fmac_f32_dpp v26, %[rA], v8 row_newbcast:10 row_mask:0xf bank_mask:0xf\n\t"
                "v_fmac_f32_dpp v27, %[rA], v8 row_newbcast:11 row_mask:0xf bank_mask:0xf\n\t"
                "v_fmac_f32_dpp v48, %[rA], v9 row_newbcast:8 row_mask:0xf bank_mask:0xf\n\t"
                "v_fmac_f32_dpp v49, %[rA], v9 row_newbcast:9 row_mask:0xf bank_mask:0xf\n\t"
                "v_fmac_f32_dpp v50, %[rA], v9 row_newbcast:10 row_mask:0xf bank_mask:0xf\n\t"
                "v_mfma_f32_16x16x32_bf16 v[64:67], v[144:147], v[136:139], v[64:67]\n\t"
                "v_fmac_f32_dpp v51, %[rA], v9 row_newbcast:11 row_mask:0xf bank_mask:0xf\n\t"
                "v_fmac_f32_dpp v72, %[rA], v10 row_newbcast:8 row_mask:0xf bank_mask:0xf\n\t"
                "v_fmac_f32_dpp v73, %[rA], v10 row_newbcast:9 row_mask:0xf bank_mask:0xf\n\t"
                "v_fmac_f32_dpp v74, %[rA], v10 row_newbcast:10 row_mask:0xf bank_mask:0xf\n\t"
                "v_fmac_f32_dpp v75, %[rA], v10 row_newbcast:11 row_mask:0xf bank_mask:0xf\n\t"
                "v_mfma_f32_16x16x32_bf16 v[88:91], v[144:147], v[140:143], v[88:91]\n\t"
                "ds_read_b64_tr_b16 v[144:145], %[b1] offset:512\n\t"
                "ds_read_b64_tr_b16 v[146:147], %[b1] offset:3584\n\t"
                "v_fmac_f32_dpp v96, %[rA], v11 row_newbcast:8 row_mask:0xf bank_mask:0xf\n\t"
                "v_fmac_f32_dpp v97, %[rA], v11 row_newbcast:9 row_mask:0xf bank_mask:0xf\n\t"
                "v_fmac_f32_dpp v98, %[rA], v11 row_newbcast:10 row_mask:0xf bank_mask:0xf\n\t"
                "v_fmac_f32_dpp v99, %[rA], v11 row_newbcast:11 row_mask:0xf bank_mask:0xf\n\t"
                "v_fmac_f32_dpp v32, %[rB], v8 row_newbcast:0 row_mask:0xf bank_mask:0xf\n\t"
                "s_waitcnt lgkmcnt(4)\n\t"
                "v_mfma_f32_16x16x32_bf16 v[20:23], v[148:151], v[128:131], v[20:23]\n\t"
                "v_fmac_f32_dpp v33, %[rB], v8 row_newbcast:1 row_mask:0xf bank_mask:0xf\n\t"
                "v_fmac_f32_dpp v34, %[rB], v8 row_newbcast:2 row_mask:0xf bank_mask:0xf\n\t"
                "v_fmac_f32_dpp v35, %[rB], v8 row_newbcast:3 row_mask:0xf bank_mask:0xf\n\t"
                "v_fmac_f32_dpp v56, %[rB], v9 row_newbcast:0 row_mask:0xf bank_mask:0xf\n\t"
                "v_fmac_f32_dpp v57, %[rB], v9 row_newbcast:1 row_mask:0xf bank_mask:0xf\n\t"
                "v_mfma_f32_16x16x32_bf16 v[44:47], v[148:151], v[132:135], v[44:47]\n\t"
                "v_fmac_f32_dpp v58, %[rB], v9 row_newbcast:2 row_mask:0xf bank_mask:0xf\n\t"
                "v_fmac_f32_dpp v59, %[rB], v9 row_newbcast:3 row_mask:0xf bank_mask:0xf\n\t"
                "v_mul_f32 v16, v12, v16\n\t"
                "v_mul_f32 v17, v12, v17\n\t"
                "v_mul_f32 v18, v12, v18\n\t"
                "v_mfma_f32_16x16x32_bf16 v[68:71], v[148:151], v[136:139], v[68:71]\n\t"
                "v_mul_f32 v19, v12, v19\n\t"
                "v_mul_f32 v20, v12, v20\n\t"
                "v_mul_f32 v21, v12, v21\n\t"
                "v_mfma_f32_16x16x32_bf16 v[92:95], v[148:151], v[140:143], v[92:95]\n\t"
                "ds_read_b64_tr_b16 v[148:149], %[b0] offset:1024\n\t"
                "ds_read_b64_tr_b16 v[150:151], %[b0] offset:4096\n\t"
                "v_mul_f32 v22, v12, v22\n\t"
                "v_mul_f32 v23, v12, v23\n\t"
                "v_cvt_pk_bf16_f32 v16, v16, v17\n\t"
                "v_mfma_f32_16x16x32_bf16 v[28:31], v[156:159], v[112:115], v[28:31]\n\t"
                "v_add_u32 %[b0], %[sH1], %[l16]\n\t"
                "v_cvt_pk_bf16_f32 v17, v18, v19\n\t"
                "v_cvt_pk_bf16_f32 v18, v20, v21\n\t"
                "v_cvt_pk_bf16_f32 v19, v22, v23\n\t"
                "v_mfma_f32_16x16x32_bf16 v[52:55], v[156:159], v[116:119], v[52:55]\n\t"
                "v_pk_max_i16 v16, v16, 0\n\t"
                "v_pk_max_i16 v17, v17, 0\n\t"
                "v_pk_max_i16 v18, v18, 0\n\t"
                "v_mfma_f32_16x16x32_bf16 v[76:79], v[156:159], v[120:123], v[76:79]\n\t"
                "v_pk_max_i16 v19, v19, 0\n\t"
                "v_fmac_f32_dpp v80, %[rB], v10 row_newbcast:0 row_mask:0xf bank_mask:0xf\n\t"
                "v_fmac_f32_dpp v81, %[rB], v10 row_newbcast:1 row_mask:0xf bank_mask:0xf\n\t"
                "v_mfma_f32_16x16x32_bf16 v[100:103], v[156:159], v[124:127], v[100:103]\n\t"
                "ds_read_b64_tr_b16 v[156:157], %[b1] offset:1024\n\t"
                "ds_read_b64_tr_b16 v[158:159], %[b1] offset:4096\n\t"
                "v_fmac_f32_dpp v82, %[rB], v10 row_newbcast:2 row_mask:0xf bank_mask:0xf\n\t"
                "v_fmac_f32_dpp v83, %[rB], v10 row_newbcast:3 row_mask:0xf bank_mask:0xf\n\t"
                "ds_write_b128 %[a], v[16:19] offset:0\n\t"
                "v_mul_f32 v40, v13, v40\n\t"
                "v_mfma_f32_16x16x32_bf16 v[32:35], v[160:163], v[112:115], v[32:35]\n\t"
                "v_mul_f32 v41, v13, v41\n\t"
                "v_mul_f32 v42, v13, v42\n\t"
                "v_mul_f32 v43, v13, v43\n\t"
                "v_mfma_f32_16x16x32_bf16 v[56:59], v[160:163], v[116:119], v[56:59]\n\t"
                "v_mul_f32 v44, v13, v44\n\t"
                "v_mul_f32 v45, v13, v45\n\t"
                "v_mul_f32 v46, v13, v46\n\t"
                "v_mfma_f32_16x16x32_bf16 v[80:83], v[160:163], v[120:123], v[80:83]\n\t"
                "v_mul_f32 v47, v13, v47\n\t"
                "v_cvt_pk_bf16_f32 v40, v40, v41\n\t"
                "v_cvt_pk_bf16_f32 v41, v42, v43\n\t"
                "v_mfma_f32_16x16x32_bf16 v[104:107], v[160:163], v[124:127], v[104:107]\n\t"
                "v_cvt_pk_bf16_f32 v42, v44, v45\n\t"
                "v_cvt_pk_bf16_f32 v43, v46, v47\n\t"
                "v_pk_max_i16 v40, v40, 0\n\t"
                "s_waitcnt lgkmcnt(1)\n\t"
                "s_barrier\n\t"
                "v_mfma_f32_16x16x32_bf16 v[24:27], v[152:155], v[128:131], v[24:27]\n\t"
                "v_pk_max_i16 v41, v41, 0\n\t"
                "v_pk_max_i16 v42, v42, 0\n\t"
                "v_pk_max_i16 v43, v43, 0\n\t"
                "v_mfma_f32_16x16x32_bf16 v[48:51], v[152:155], v[132:135], v[48:51]\n\t"
                "ds_write_b128 %[a], v[40:43] offset:2048\n\t"
                "v_mul_f32 v64, v14, v64\n\t"
                "v_mul_f32 v65, v14, v65\n\t"
                "v_mul_f32 v66, v14, v66\n\t"
                "v_mfma_f32_16x16x32_bf16 v[72:75], v[152:155], v[136:139], v[72:75]\n\t"
                "v_mul_f32 v67, v14, v67\n\t"
                "v_mul_f32 v68, v14, v68\n\t"
                "v_mul_f32 v69, v14, v69\n\t"
                "v_mfma_f32_16x16x32_bf16 v[96:99], v[152:155], v[140:143], v[96:99]\n\t"
                "v_mul_f32 v70, v14, v70\n\t"
                "v_mul_f32 v71, v14, v71\n\t"
                "v_cvt_pk_bf16_f32 v64, v64, v65\n\t"
                "v_mfma_f32_16x16x32_bf16 v[28:31], v[144:147], v[128:131], v[28:31]\n\t"
                "v_cvt_pk_bf16_f32 v65, v66, v67\n\t"
                "v_cvt_pk_bf16_f32 v66, v68, v69\n\t"
                "v_cvt_pk_bf16_f32 v67, v70, v71\n\t"
                "v_mfma_f32_16x16x32_bf16 v[52:55], v[144:147], v[132:135], v[52:55]\n\t"
                "v_pk_max_i16 v64, v64, 0\n\t"
                "v_pk_max_i16 v65, v65, 0\n\t"
                "v_pk_max_i16 v66, v66, 0\n\t"
                "v_mfma_f32_16x16x32_bf16 v[76:79], v[144:147], v[136:139], v[76:79]\n\t"
                "v_pk_max_i16 v67, v67, 0\n\t"
                "ds_write_b128 %[a], v[64:67] offset:4096\n\t"
                "v_mul_f32 v88, v15, v88\n\t"
                "v_mul_f32 v89, v15, v89\n\t"
                "v_mfma_f32_16x16x32_bf16 v[100:103], v[144:147], v[140:143], v[100:103]\n\t"
                "v_mul_f32 v90, v15, v90\n\t"
                "v_mul_f32 v91, v15, v91\n\t"
                "v_mul_f32 v92, v15, v92\n\t"
                "v_mfma_f32_16x16x32_bf16 v[36:39], v[164:167], v[112:115], v[36:39]\n\t"
                "v_mul_f32 v93, v15, v93\n\t"
                "v_mul_f32 v94, v15, v94\n\t"
                "v_mul_f32 v95, v15, v95\n\t"
                "v_mfma_f32_16x16x32_bf16 v[60:63], v[164:167], v[116:119], v[60:63]\n\t"
                "v_cvt_pk_bf16_f32 v88, v88, v89\n\t"
                "v_cvt_pk_bf16_f32 v89, v90, v91\n\t"
                "v_cvt_pk_bf16_f32 v90, v92, v93\n\t"
                "v_mfma_f32_16x16x32_bf16 v[84:87], v[164:167], v[120:123], v[84:87]\n\t"
                "v_cvt_pk_bf16_f32 v91, v94, v95\n\t"
                "v_pk_max_i16 v88, v88, 0\n\t"
                "v_pk_max_i16 v89, v89, 0\n\t"
                "v_mfma_f32_16x16x32_bf16 v[108:111], v[164:167], v[124:127], v[108:111]\n\t"
                "v_pk_max_i16 v90, v90, 0\n\t"
                "v_pk_max_i16 v91, v91, 0\n\t"
                "ds_write_b128 %[a], v[88:91] offset:6144\n\t"
                "v_mul_f32 v24, v12, v24\n\t"
                "v_mfma_f32_16x16x32_bf16 v[32:35], v[148:151], v[128:131], v[32:35]\n\t"
                "v_mul_f32 v25, v12, v25\n\t"
                "v_mul_f32 v26, v12, v26\n\t"
                "v_mul_f32 v27, v12, v27\n\t"
                "v_mfma_f32_16x16x32_bf16 v[56:59], v[148:151], v[132:135], v[56:59]\n\t"
                "v_mul_f32 v28, v12, v28\n\t"
                "v_mul_f32 v29, v12, v29\n\t"
                "v_mul_f32 v30, v12, v30\n\t"
                "v_mfma_f32_16x16x32_bf16 v[80:83], v[148:151], v[136:139], v[80:83]\n\t"
                "v_mul_f32 v31, v12, v31\n\t"
                "v_cvt_pk_bf16_f32 v24, v24, v25\n\t"
                "v_cvt_pk_bf16_f32 v25, v26, v27\n\t"
                "v_mfma_f32_16x16x32_bf16 v[104:107], v[148:151], v[140:143], v[104:107]\n\t"
                "v_cvt_pk_bf16_f32 v26, v28, v29\n\t"
                "v_cvt_pk_bf16_f32 v27, v30, v31\n\t"
                "v_pk_max_i16 v24, v24, 0\n\t"
                "v_mfma_f32_16x16x32_bf16 v[36:39], v[156:159], v[128:131], v[36:39]\n\t"
                "v_pk_max_i16 v25, v25, 0\n\t"
                "v_pk_max_i16 v26, v26, 0\n\t"
                "v_pk_max_i16 v27, v27, 0\n\t"
                "v_mfma_f32_16x16x32_bf16 v[60:63], v[156:159], v[132:135], v[60:63]\n\t"
                "ds_write_b128 %[b0], v[24:27] offset:0\n\t"
                "v_mul_f32 v48, v13, v48\n\t"
                "v_mul_f32 v49, v13, v49\n\t"
                "v_mul_f32 v50, v13, v50\n\t"
                "v_mfma_f32_16x16x32_bf16 v[84:87], v[156:159], v[136:139], v[84:87]\n\t"
                "v_mul_f32 v51, v13, v51\n\t"
                "v_mul_f32 v52, v13, v52\n\t"
                "v_mul_f32 v53, v13, v53\n\t"
                "v_mfma_f32_16x16x32_bf16 v[108:111], v[156:159], v[140:143], v[108:111]\n\t"
                "v_mul_f32 v54, v13, v54\n\t"
                "v_mul_f32 v55, v13, v55\n\t"
                "v_cvt_pk_bf16_f32 v48, v48, v49\n\t"
                "v_cvt_pk_bf16_f32 v49, v50, v51\n\t"
                "v_cvt_pk_bf16_f32 v50, v52, v53\n\t"
                "v_cvt_pk_bf16_f32 v51, v54, v55\n\t"
                "v_pk_max_i16 v48, v48, 0\n\t"
                "v_pk_max_i16 v49, v49, 0\n\t"
                "v_pk_max_i16 v50, v50, 0\n\t"
                "v_pk_max_i16 v51, v51, 0\n\t"
                "ds_write_b128 %[b0], v[48:51] offset:2048\n\t"
                "v_pk_mul_f32 v[72:73], v[14:15], v[72:73] op_sel_hi:[0,1]\n\t"
                "v_pk_mul_f32 v[74:75], v[14:15], v[74:75] op_sel_hi:[0,1]\n\t"
                "v_pk_mul_f32 v[76:77], v[14:15], v[76:77] op_sel_hi:[0,1]\n\t"
                "v_pk_mul_f32 v[78:79], v[14:15], v[78:79] op_sel_hi:[0,1]\n\t"
                "v_cvt_pk_bf16_f32 v72, v72, v73\n\t"
                "v_cvt_pk_bf16_f32 v73, v74, v75\n\t"
                "v_cvt_pk_bf16_f32 v74, v76, v77\n\t"
                "v_cvt_pk_bf16_f32 v75, v78, v79\n\t"
                "v_pk_max_i16 v72, v72, 0\n\t"
                "v_pk_max_i16 v73, v73, 0\n\t"
                "v_pk_max_i16 v74, v74, 0\n\t"
                "v_pk_max_i16 v75, v75, 0\n\t"
                "ds_write_b128 %[b0], v[72:75] offset:4096\n\t"
                "v_pk_mul_f32 v[96:97], v[14:15], v[96:97] op_sel:[1,0] op_sel_hi:[1,1]\n\t"
                "v_pk_mul_f32 v[98:99], v[14:15], v[98:99] op_sel:[1,0] op_sel_hi:[1,1]\n\t"
                "v_pk_mul_f32 v[100:101], v[14:15], v[100:101] op_sel:[1,0] op_sel_hi:[1,1]\n\t"
                "v_pk_mul_f32 v[102:103], v[14:15], v[102:103] op_sel:[1,0] op_sel_hi:[1,1]\n\t"
                "v_cvt_pk_bf16_f32 v96, v96, v97\n\t"
                "v_cvt_pk_bf16_f32 v97, v98, v99\n\t"
                "v_cvt_pk_bf16_f32 v98, v100, v101\n\t"
                "v_cvt_pk_bf16_f32 v99, v102, v103\n\t"
                "v_pk_max_i16 v96, v96, 0\n\t"
                "v_pk_max_i16 v97, v97, 0\n\t"
                "v_pk_max_i16 v98, v98, 0\n\t"
                "v_pk_max_i16 v99, v99, 0\n\t"
                "ds_write_b128 %[b0], v[96:99] offset:6144\n\t"
                "v_fmac_f32_dpp v104, %[rB], v11 row_newbcast:0 row_mask:0xf bank_mask:0xf\n\t"
                "v_fmac_f32_dpp v105, %[rB], v11 row_newbcast:1 row_mask:0xf bank_mask:0xf\n\t"
                "v_fmac_f32_dpp v106, %[rB], v11 row_newbcast:2 row_mask:0xf bank_mask:0xf\n\t"
                "v_fmac_f32_dpp v107, %[rB], v11 row_newbcast:3 row_mask:0xf bank_mask:0xf\n\t"
                "v_fmac_f32_dpp v36, %[rB], v8 row_newbcast:4 row_mask:0xf bank_mask:0xf\n\t"
                "v_fmac_f32_dpp v37, %[rB], v8 row_newbcast:5 row_mask:0xf bank_mask:0xf\n\t"
                "v_fmac_f32_dpp v38, %[rB], v8 row_newbcast:6 row_mask:0xf bank_mask:0xf\n\t"
                "v_fmac_f32_dpp v39, %[rB], v8 row_newbcast:7 row_mask:0xf bank_mask:0xf\n\t"
                "v_pk_mul_f32 v[32:33], v[12:13], v[32:33] op_sel_hi:[0,1]\n\t"
                "v_pk_mul_f32 v[34:35], v[12:13], v[34:35] op_sel_hi:[0,1]\n\t"
                "v_pk_mul_f32 v[36:37], v[12:13], v[36:37] op_sel_hi:[0,1]\n\t"
                "v_pk_mul_f32 v[38:39], v[12:13], v[38:39] op_sel_hi:[0,1]\n\t"
                "v_cvt_pk_bf16_f32 v32, v32, v33\n\t"
                "v_cvt_pk_bf16_f32 v33, v34, v35\n\t"
                "v_cvt_pk_bf16_f32 v34, v36, v37\n\t"
                "v_cvt_pk_bf16_f32 v35, v38, v39\n\t"
                "v_pk_max_i16 v32, v32, 0\n\t"
                "v_pk_max_i16 v33, v33, 0\n\t"
                "v_pk_max_i16 v34, v34, 0\n\t"
                "v_pk_max_i16 v35, v35, 0\n\t"
                "ds_write_b128 %[a], v[32:35] offset:32768\n\t"
                "v_fmac_f32_dpp v60, %[rB], v9 row_newbcast:4 row_mask:0xf bank_mask:0xf\n\t"
                "v_fmac_f32_dpp v61, %[rB], v9 row_newbcast:5 row_mask:0xf bank_mask:0xf\n\t"
                "v_fmac_f32_dpp v62, %[rB], v9 row_newbcast:6 row_mask:0xf bank_mask:0xf\n\t"
                "v_fmac_f32_dpp v63, %[rB], v9 row_newbcast:7 row_mask:0xf bank_mask:0xf\n\t"
                "v_pk_mul_f32 v[56:57], v[12:13], v[56:57] op_sel:[1,0] op_sel_hi:[1,1]\n\t"
                "v_pk_mul_f32 v[58:59], v[12:13], v[58:59] op_sel:[1,0] op_sel_hi:[1,1]\n\t"
                "v_pk_mul_f32 v[60:61], v[12:13], v[60:61] op_sel:[1,0] op_sel_hi:[1,1]\n\t"
                "v_pk_mul_f32 v[62:63], v[12:13], v[62:63] op_sel:[1,0] op_sel_hi:[1,1]\n\t"
                "v_cvt_pk_bf16_f32 v56, v56, v57\n\t"
                "v_cvt_pk_bf16_f32 v57, v58, v59\n\t"
                "v_cvt_pk_bf16_f32 v58, v60, v61\n\t"
                "v_cvt_pk_bf16_f32 v59, v62, v63\n\t"
                "v_pk_max_i16 v56, v56, 0\n\t"
                "v_pk_max_i16 v57, v57, 0\n\t"
                "v_pk_max_i16 v58, v58, 0\n\t"
                "v_pk_max_i16 v59, v59, 0\n\t"
                "ds_write_b128 %[a], v[56:59] offset:34816\n\t"
                "v_fmac_f32_dpp v84, %[rB], v10 row_newbcast:4 row_mask:0xf bank_mask:0xf\n\t"
                "v_fmac_f32_dpp v85, %[rB], v10 row_newbcast:5 row_mask:0xf bank_mask:0xf\n\t"
                "v_fmac_f32_dpp v86, %[rB], v10 row_newbcast:6 row_mask:0xf bank_mask:0xf\n\t"
                "v_fmac_f32_dpp v87, %[rB], v10 row_newbcast:7 row_mask:0xf bank_mask:0xf\n\t"
                "v_pk_mul_f32 v[80:81], v[14:15], v[80:81] op_sel_hi:[0,1]\n\t"
                "v_pk_mul_f32 v[82:83], v[14:15], v[82:83] op_sel_hi:[0,1]\n\t"
                "v_pk_mul_f32 v[84:85], v[14:15], v[84:85] op_sel_hi:[0,1]\n\t"
                "v_pk_mul_f32 v[86:87], v[14:15], v[86:87] op_sel_hi:[0,1]\n\t"
                "v_cvt_pk_bf16_f32 v80, v80, v81\n\t"
                "v_cvt_pk_bf16_f32 v81, v82, v83\n\t"
                "v_cvt_pk_bf16_f32 v82, v84, v85\n\t"
                "v_cvt_pk_bf16_f32 v83, v86, v87\n\t"
                "v_pk_max_i16 v80, v80, 0\n\t"
                "v_pk_max_i16 v81, v81, 0\n\t"
                "v_pk_max_i16 v82, v82, 0\n\t"
                "v_pk_max_i16 v83, v83, 0\n\t"
                "ds_write_b128 %[a], v[80:83] offset:36864\n\t"
                "v_fmac_f32_dpp v108, %[rB], v11 row_newbcast:4 row_mask:0xf bank_mask:0xf\n\t"
                "v_fmac_f32_dpp v109, %[rB], v11 row_newbcast:5 row_mask:0xf bank_mask:0xf\n\t"
                "v_fmac_f32_dpp v110, %[rB], v11 row_newbcast:6 row_mask:0xf bank_mask:0xf\n\t"
                "v_fmac_f32_dpp v111, %[rB], v11 row_newbcast:7 row_mask:0xf bank_mask:0xf\n\t"
                "v_pk_mul_f32 v[104:105], v[14:15], v[104:105] op_sel:[1,0] op_sel_hi:[1,1]\n\t"
                "v_pk_mul_f32 v[106:107], v[14:15], v[106:107] op_sel:[1,0] op_sel_hi:[1,1]\n\t"
                "v_pk_mul_f32 v[108:109], v[14:15], v[108:109] op_sel:[1,0] op_sel_hi:[1,1]\n\t"
                "v_pk_mul_f32 v[110:111], v[14:15], v[110:111] op_sel:[1,0] op_sel_hi:[1,1]\n\t"
                "v_cvt_pk_bf16_f32 v104, v104, v105\n\t"
                "v_cvt_pk_bf16_f32 v105, v106, v107\n\t"
                "v_cvt_pk_bf16_f32 v106, v108, v109\n\t"
                "v_cvt_pk_bf16_f32 v107, v110, v111\n\t"
                "v_pk_max_i16 v104, v104, 0\n\t"
                "v_pk_max_i16 v105, v105, 0\n\t"
                "v_pk_max_i16 v106, v106, 0\n\t"
                "v_pk_max_i16 v107, v107, 0\n\t"
                "ds_write_b128 %[a], v[104:107] offset:38912\n\t"
                : [a] "=&v"(a), [b0] "=&v"(b0), [b1] "=&v"(b1), [sA] "+&s"(sA), [sB] "+&s"(sB), [sN] "+&s"(sN), [rA] "+&v"(rA), [rB] "+&v"(rB)
                : [l16] "v"(lv), [sH0] "s"(sH0), [sH1] "s"(sH1), [sSt] "s"(sSt), [sCs] "s"(sCs)
                : "memory", "scc", "v8", "v9", "v10", "v11", "v12", "v13", "v14", "v15", "v16", "v17", "v18", "v19", "v20", "v21", "v22", "v23", "v24", "v25", "v26", "v27", "v28", "v29", "v30", "v31", "v32", "v33", "v34", "v35", "v36", "v37", "v38", "v39", "v40", "v41", "v42", "v43", "v44", "v45", "v46", "v47", "v48", "v49", "v50", "v51", "v52", "v53", "v54", "v55", "v56", "v57", "v58", "v59", "v60", "v61", "v62", "v63", "v64", "v65", "v66", "v67", "v68", "v69", "v70", "v71", "v72", "v73", "v74", "v75", "v76", "v77", "v78", "v79", "v80", "v81", "v82", "v83", "v84", "v85", "v86", "v87", "v88", "v89", "v90", "v91", "v92", "v93", "v94", "v95", "v96", "v97", "v98", "v99", "v100", "v101", "v102", "v103", "v104", "v105", "v106", "v107", "v108", "v109", "v110", "v111", "v112", "v113", "v114", "v115", "v116", "v117", "v118", "v119", "v120", "v121", "v122", "v123", "v124", "v125", "v126", "v127", "v128", "v129", "v130", "v131", "v132", "v133", "v134", "v135", "v136", "v137", "v138", "v139", "v140", "v141", "v142", "v143", "v144", "v145", "v146", "v147", "v148", "v149", "v150", "v151", "v152", "v153", "v154", "v155", "v156", "v157", "v158", "v159", "v160", "v161", "v162", "v163", "v164", "v165", "v166", "v167");
            PIN(lv);
            STAMP(2);
        } else {
            const u32 wA = (u32)wm * 8192u + (u32)wn * 4096u, sD = (u32)wn * 8192u - 16384u, sSt = L_STAT + (u32)wm * 256u + (u32)wn * 128u, sCs = L_CS + (u32)wn * 256u;
            const u32 ob = wm == 3 ? WS_AB + (u32)((j * 4 + cn) * 16 + 8 * (mb >> 2) + 6) * 9216u : L_B + (u32)((wm + 2) % 3) * 18432u;
            const u32 o0 = ob + (u32)wn * 13312u, o1 = ob + 9216u - (u32)wn * 5120u, o8 = ob + (u32)wn * 9216u + 8192u;
            u32 a01, a23, b0, b8, sA = wA, sS = L_B + 16384u, sB = sS + sD, sN = 9;
            asm volatile(
                "v_mov_b32 v24, 0\n\t"
                "v_mov_b32 v25, 0\n\t"
                "v_mov_b32 v26, 0\n\t"
                "v_mov_b32 v27, 0\n\t"
                "v_mov_b32 v28, 0\n\t"
                "v_mov_b32 v29, 0\n\t"
                "v_mov_b32 v30, 0\n\t"
                "v_mov_b32 v31, 0\n\t"
                "v_mov_b32 v32, 0\n\t"
                "v_mov_b32 v33, 0\n\t"
                "v_mov_b32 v34, 0\n\t"
                "v_mov_b32 v35, 0\n\t"
                "v_mov_b32 v36, 0\n\t"
                "v_mov_b32 v37, 0\n\t"
                "v_mov_b32 v38, 0\n\t"
                "v_mov_b32 v39, 0\n\t"
                "v_mov_b32 v40, 0\n\t"
                "v_mov_b32 v41, 0\n\t"
                "v_mov_b32 v42, 0\n\t"
                "v_mov_b32 v43, 0\n\t"
                "v_mov_b32 v44, 0\n\t"
                "v_mov_b32 v45, 0\n\t"
                "v_mov_b32 v46, 0\n\t"
                "v_mov_b32 v47, 0\n\t"
                "v_mov_b32 v48, 0\n\t"
                "v_mov_b32 v49, 0\n\t"
                "v_mov_b32 v50, 0\n\t"
                "v_mov_b32 v51, 0\n\t"
                "v_mov_b32 v52, 0\n\t"
                "v_mov_b32 v53, 0\n\t"
                "v_mov_b32 v54, 0\n\t"
                "v_mov_b32 v55, 0\n\t"
                "v_mov_b32 v56, 0\n\t"
                "v_mov_b32 v57, 0\n\t"
                "v_mov_b32 v58, 0\n\t"
                "v_mov_b32 v59, 0\n\t"
                "v_mov_b32 v60, 0\n\t"
                "v_mov_b32 v61, 0\n\t"
                "v_mov_b32 v62, 0\n\t"
                "v_mov_b32 v63, 0\n\t"
                "v_mov_b32 v64, 0\n\t"
                "v_mov_b32 v65, 0\n\t"
                "v_mov_b32 v66, 0\n\t"
                "v_mov_b32 v67, 0\n\t"
                "v_mov_b32 v68, 0\n\t"
                "v_mov_b32 v69, 0\n\t"
                "v_mov_b32 v70, 0\n\t"
                "v_mov_b32 v71, 0\n\t"
                "v_mov_b32 v72, 0\n\t"
                "v_mov_b32 v73, 0\n\t"
                "v_mov_b32 v74, 0\n\t"
                "v_mov_b32 v75, 0\n\t"
                "v_mov_b32 v76, 0\n\t"
                "v_mov_b32 v77, 0\n\t"
                "v_mov_b32 v78, 0\n\t"
                "v_mov_b32 v79, 0\n\t"
                "v_mov_b32 v80, 0\n\t"
                "v_mov_b32 v81, 0\n\t"
                "v_mov_b32 v82, 0\n\t"
                "v_mov_b32 v83, 0\n\t"
                "v_mov_b32 v84, 0\n\t"
                "v_mov_b32 v85, 0\n\t"
                "v_mov_b32 v86, 0\n\t"
                "v_mov_b32 v87, 0\n\t"
                "v_mov_b32 v88, 0\n\t"
                "v_mov_b32 v89, 0\n\t"
                "v_mov_b32 v90, 0\n\t"
                "v_mov_b32 v91, 0\n\t"
                "v_mov_b32 v92, 0\n\t"
                "v_mov_b32 v93, 0\n\t"
                "v_mov_b32 v94, 0\n\t"
                "v_mov_b32 v95, 0\n\t"
                "v_add_u32 %[a01], %[sA], %[rowl]\n\t"
                "v_xor_b32 %[a23], 0x1000, %[a01]\n\t"
                "v_add_u32 %[b0], %[sB], %[rowl]\n\t"
                "v_add_u32 %[b8], %[sS], %[rowl]\n\t"
                "s_barrier\n\t"
                "ds_read_b128 v[96:99], %[a01] offset:0\n\t"
                "ds_read_b128 v[100:103], %[a01] offset:2048\n\t"
                "ds_read_b128 v[104:107], %[a23] offset:0\n\t"
                "ds_read_b128 v[108:111], %[a23] offset:2048\n\t"
                "ds_read_b128 v[128:131], %[b0] offset:0\n\t"
                "ds_read_b128 v[132:135], %[b0] offset:2048\n\t"
                "ds_read_b128 v[136:139], %[b0] offset:4096\n\t"
                "ds_read_b128 v[140:143], %[b0] offset:6144\n\t"
                "ds_read_b128 v[144:147], %[b8]\n\t"
                "s_branch LW%=\n\t"
                ".p2align 6\n\t"
                "L1%=:\n\t"
                "v_mfma_f32_16x16x32_bf16 v[68:71], v[120:123], v[160:163], v[68:71]\n\t"
                "v_mfma_f32_16x16x32_bf16 v[84:87], v[124:127], v[160:163], v[84:87]\n\t"
                "v_mfma_f32_16x16x32_bf16 v[88:91], v[112:115], v[164:167], v[88:91]\n\t"
                "v_mfma_f32_16x16x32_bf16 v[92:95], v[116:119], v[164:167], v[92:95]\n\t"
                "LW%=:\n\t"
                "s_waitcnt lgkmcnt(0)\n\t"
                "v_mfma_f32_16x16x32_bf16 v[24:27], v[96:99], v[128:131], v[24:27]\n\t"
                "v_xor_b32 %[a01], 64, %[a01]\n\t"
                "v_xor_b32 %[a23], 64, %[a23]\n\t"
                "ds_read_b128 v[112:115], %[a01] offset:0\n\t"
                "v_mfma_f32_16x16x32_bf16 v[40:43], v[100:103], v[128:131], v[40:43]\n\t"
                "v_xor_b32 %[b0], 64, %[b0]\n\t"
                "v_xor_b32 %[b8], 64, %[b8]\n\t"
                "ds_read_b128 v[116:119], %[a01] offset:2048\n\t"
                "v_mfma_f32_16x16x32_bf16 v[56:59], v[104:107], v[128:131], v[56:59]\n\t"
                "ds_read_b128 v[120:123], %[a23] offset:0\n\t"
                "v_mfma_f32_16x16x32_bf16 v[72:75], v[108:111], v[128:131], v[72:75]\n\t"
                "ds_read_b128 v[124:127], %[a23] offset:2048\n\t"
                "v_mfma_f32_16x16x32_bf16 v[28:31], v[96:99], v[132:135], v[28:31]\n\t"
                "ds_read_b128 v[148:151], %[b0] offset:0\n\t"
                "v_mfma_f32_16x16x32_bf16 v[44:47], v[100:103], v[132:135], v[44:47]\n\t"
                "ds_read_b128 v[152:155], %[b0] offset:2048\n\t"
                "v_mfma_f32_16x16x32_bf16 v[60:63], v[104:107], v[132:135], v[60:63]\n\t"
                "ds_read_b128 v[156:159], %[b0] offset:4096\n\t"
                "v_mfma_f32_16x16x32_bf16 v[76:79], v[108:111], v[132:135], v[76:79]\n\t"
                "ds_read_b128 v[160:163], %[b0] offset:6144\n\t"
                "v_mfma_f32_16x16x32_bf16 v[32:35], v[96:99], v[136:139], v[32:35]\n\t"
                "ds_read_b128 v[164:167], %[b8]\n\t"
                "v_mfma_f32_16x16x32_bf16 v[48:51], v[100:103], v[136:139], v[48:51]\n\t"
                "s_add_u32 %[sA], %[sA], 0x8000\n\t"
                "s_cmp_lt_u32 %[sA], 0x18000\n\t"
                "s_cselect_b32 %[sA], %[sA], %[wA]\n\t"
                "v_mfma_f32_16x16x32_bf16 v[64:67], v[104:107], v[136:139], v[64:67]\n\t"
                "s_add_u32 %[sS], %[sS], 18432\n\t"
                "s_cmp_lt_u32 %[sS], 169984\n\t"
                "s_cselect_b32 %[sS], %[sS], 114688\n\t"
                "v_mfma_f32_16x16x32_bf16 v[80:83], v[108:111], v[136:139], v[80:83]\n\t"
                "s_add_u32 %[sB], %[sS], %[sD]\n\t"
                "s_sub_u32 %[sN], %[sN], 1\n\t"
                "s_cmp_eq_u32 %[sN], 0\n\t"
                "v_mfma_f32_16x16x32_bf16 v[36:39], v[96:99], v[140:143], v[36:39]\n\t"
                "s_cbranch_scc1 LX%=\n\t"
                "v_mfma_f32_16x16x32_bf16 v[52:55], v[100:103], v[140:143], v[52:55]\n\t"
                "v_mfma_f32_16x16x32_bf16 v[68:71], v[104:107], v[140:143], v[68:71]\n\t"
                "v_mfma_f32_16x16x32_bf16 v[84:87], v[108:111], v[140:143], v[84:87]\n\t"
                "v_mfma_f32_16x16x32_bf16 v[88:91], v[96:99], v[144:147], v[88:91]\n\t"
                "v_mfma_f32_16x16x32_bf16 v[92:95], v[100:103], v[144:147], v[92:95]\n\t"
                "s_waitcnt lgkmcnt(0)\n\t"
                "s_barrier\n\t"
                "v_mfma_f32_16x16x32_bf16 v[24:27], v[112:115], v[148:151], v[24:27]\n\t"
                "v_add_u32 %[a01], %[sA], %[rowl]\n\t"
                "v_xor_b32 %[a23], 0x1000, %[a01]\n\t"
                "ds_read_b128 v[96:99], %[a01] offset:0\n\t"
                "v_mfma_f32_16x16x32_bf16 v[40:43], v[116:119], v[148:151], v[40:43]\n\t"
                "v_add_u32 %[b0], %[sB], %[rowl]\n\t"
                "v_add_u32 %[b8], %[sS], %[rowl]\n\t"
                "ds_read_b128 v[100:103], %[a01] offset:2048\n\t"
                "v_mfma_f32_16x16x32_bf16 v[56:59], v[120:123], v[148:151], v[56:59]\n\t"
                "ds_read_b128 v[104:107], %[a23] offset:0\n\t"
                "v_mfma_f32_16x16x32_bf16 v[72:75], v[124:127], v[148:151], v[72:75]\n\t"
                "ds_read_b128 v[108:111], %[a23] offset:2048\n\t"
                "v_mfma_f32_16x16x32_bf16 v[28:31], v[112:115], v[152:155], v[28:31]\n\t"
                "ds_read_b128 v[128:131], %[b0] offset:0\n\t"
                "v_mfma_f32_16x16x32_bf16 v[44:47], v[116:119], v[152:155], v[44:47]\n\t"
                "ds_read_b128 v[132:135], %[b0] offset:2048\n\t"
                "v_mfma_f32_16x16x32_bf16 v[60:63], v[120:123], v[152:155], v[60:63]\n\t"
                "ds_read_b128 v[136:139], %[b0] offset:4096\n\t"
                "v_mfma_f32_16x16x32_bf16 v[76:79], v[124:127], v[152:155], v[76:79]\n\t"
                "ds_read_b128 v[140:143], %[b0] offset:6144\n\t"
                "v_mfma_f32_16x16x32_bf16 v[32:35], v[112:115], v[156:159], v[32:35]\n\t"
                "ds_read_b128 v[144:147], %[b8]\n\t"
                "v_mfma_f32_16x16x32_bf16 v[48:51], v[116:119], v[156:159], v[48:51]\n\t"
                "v_mfma_f32_16x16x32_bf16 v[64:67], v[120:123], v[156:159], v[64:67]\n\t"
                "v_mfma_f32_16x16x32_bf16 v[80:83], v[124:127], v[156:159], v[80:83]\n\t"
                "v_mfma_f32_16x16x32_bf16 v[36:39], v[112:115], v[160:163], v[36:39]\n\t"
                "v_mfma_f32_16x16x32_bf16 v[52:55], v[116:119], v[160:163], v[52:55]\n\t"
                "s_branch L1%=\n\t"
                "LX%=:\n\t"
                "v_mfma_f32_16x16x32_bf16 v[52:55], v[100:103], v[140:143], v[52:55]\n\t"
                "s_cmp_eq_u32 %[sG], 3\n\t"
                "v_mfma_f32_16x16x32_bf16 v[68:71], v[104:107], v[140:143], v[68:71]\n\t"
                "s_cbranch_scc1 LG%=\n\t"
                "v_mfma_f32_16x16x32_bf16 v[84:87], v[108:111], v[140:143], v[84:87]\n\t"
                "v_lshrrev_b32 %[a01], 3, %[l16]\n\t"
                "v_and_b32 %[a01], 0x60, %[a01]\n\t"
                "v_add_u32 %[a01], %[sSt], %[a01]\n\t"
                "v_mfma_f32_16x16x32_bf16 v[88:91], v[96:99], v[144:147], v[88:91]\n\t"
                "v_xor_b32 %[a23], 0x80, %[a01]\n\t"
                "v_lshrrev_b32 %[b8], 2, %[l16]\n\t"
                "v_and_b32 %[b8], 60, %[b8]\n\t"
                "v_mfma_f32_16x16x32_bf16 v[92:95], v[100:103], v[144:147], v[92:95]\n\t"
                "v_add_u32 %[b0], %[sCs], %[b8]\n\t"
                "s_waitcnt lgkmcnt(0)\n\t"
                "s_barrier\n\t"
                "v_or_b32 %[b8], 0x20000, %[b8]\n\t"
                "v_mfma_f32_16x16x32_bf16 v[24:27], v[112:115], v[148:151], v[24:27]\n\t"
                "ds_read2_b32 v[18:19], %[b0] offset0:0 offset1:16\n\t"
                "ds_read_b128 v[96:99], %[a01] offset:0\n\t"
                "v_mfma_f32_16x16x32_bf16 v[40:43], v[116:119], v[148:151], v[40:43]\n\t"
                "ds_read_b128 v[100:103], %[a01] offset:16\n\t"
                "ds_read_b128 v[104:107], %[a01] offset:1024\n\t"
                "v_mfma_f32_16x16x32_bf16 v[56:59], v[120:123], v[148:151], v[56:59]\n\t"
                "ds_read_b128 v[108:111], %[a01] offset:1040\n\t"
                "ds_read_b128 v[128:131], %[a23] offset:0\n\t"
                "v_mfma_f32_16x16x32_bf16 v[72:75], v[124:127], v[148:151], v[72:75]\n\t"
                "ds_read_b128 v[132:135], %[a23] offset:16\n\t"
                "ds_read_b128 v[136:139], %[a23] offset:1024\n\t"
                "v_mfma_f32_16x16x32_bf16 v[28:31], v[112:115], v[152:155], v[28:31]\n\t"
                "ds_read_b128 v[140:143], %[a23] offset:1040\n\t"
                "ds_read2_b32 v[20:21], %[b0] offset0:32 offset1:48\n\t"
                "v_mfma_f32_16x16x32_bf16 v[44:47], v[116:119], v[152:155], v[44:47]\n\t"
                "ds_read_b32 v22, %[b8] offset:31232\n\t"
                "v_add_u32 %[a01], %[o0], %[l16]\n\t"
                "v_add_u32 %[a23], %[o1], %[l16]\n\t"
                "v_add_u32 %[b0], %[o8], %[l16]\n\t"
                "s_waitcnt lgkmcnt(0)\n\t"
                "v_mfma_f32_16x16x32_bf16 v[60:63], v[120:123], v[152:155], v[60:63]\n\t"
                "v_fmac_f32 v24, v96, v18\n\t"
                "v_fmac_f32 v25, v97, v18\n\t"
                "v_fmac_f32 v26, v98, v18\n\t"
                "v_mfma_f32_16x16x32_bf16 v[76:79], v[124:127], v[152:155], v[76:79]\n\t"
                "v_fmac_f32 v27, v99, v18\n\t"
                "v_fmac_f32 v40, v100, v18\n\t"
                "v_fmac_f32 v41, v101, v18\n\t"
                "v_mfma_f32_16x16x32_bf16 v[32:35], v[112:115], v[156:159], v[32:35]\n\t"
                "v_fmac_f32 v42, v102, v18\n\t"
                "v_fmac_f32 v43, v103, v18\n\t"
                "v_mul_f32 v24, v104, v24\n\t"
                "v_mfma_f32_16x16x32_bf16 v[48:51], v[116:119], v[156:159], v[48:51]\n\t"
                "v_mul_f32 v25, v105, v25\n\t"
                "v_mul_f32 v26, v106, v26\n\t"
                "v_mul_f32 v27, v107, v27\n\t"
                "v_mfma_f32_16x16x32_bf16 v[64:67], v[120:123], v[156:159], v[64:67]\n\t"
                "v_mul_f32 v40, v108, v40\n\t"
                "v_mul_f32 v41, v109, v41\n\t"
                "v_mul_f32 v42, v110, v42\n\t"
                "v_mfma_f32_16x16x32_bf16 v[80:83], v[124:127], v[156:159], v[80:83]\n\t"
                "v_mul_f32 v43, v111, v43\n\t"
                "v_cvt_pk_bf16_f32 v24, v24, v25\n\t"
                "v_cvt_pk_bf16_f32 v25, v26, v27\n\t"
                "v_mfma_f32_16x16x32_bf16 v[36:39], v[112:115], v[160:163], v[36:39]\n\t"
                "v_cvt_pk_bf16_f32 v26, v40, v41\n\t"
                "v_cvt_pk_bf16_f32 v27, v42, v43\n\t"
                "ds_write_b128 %[a01], v[24:27] offset:0\n\t"
                "v_fmac_f32 v56, v128, v18\n\t"
                "v_mfma_f32_16x16x32_bf16 v[52:55], v[116:119], v[160:163], v[52:55]\n\t"
                "v_fmac_f32 v57, v129, v18\n\t"
                "v_fmac_f32 v58, v130, v18\n\t"
                "v_fmac_f32 v59, v131, v18\n\t"
                "v_mfma_f32_16x16x32_bf16 v[68:71], v[120:123], v[160:163], v[68:71]\n\t"
                "v_fmac_f32 v72, v132, v18\n\t"
                "v_fmac_f32 v73, v133, v18\n\t"
                "v_fmac_f32 v74, v134, v18\n\t"
                "v_mfma_f32_16x16x32_bf16 v[84:87], v[124:127], v[160:163], v[84:87]\n\t"
                "v_fmac_f32 v75, v135, v18\n\t"
                "v_mul_f32 v56, v136, v56\n\t"
                "v_mul_f32 v57, v137, v57\n\t"
                "v_mfma_f32_16x16x32_bf16 v[88:91], v[112:115], v[164:167], v[88:91]\n\t"
                "v_mul_f32 v58, v138, v58\n\t"
                "v_mul_f32 v59, v139, v59\n\t"
                "v_mul_f32 v72, v140, v72\n\t"
                "v_mfma_f32_16x16x32_bf16 v[92:95], v[116:119], v[164:167], v[92:95]\n\t"
                "v_mul_f32 v73, v141, v73\n\t"
                "v_mul_f32 v74, v142, v74\n\t"
                "v_mul_f32 v75, v143, v75\n\t"
                "v_cvt_pk_bf16_f32 v56, v56, v57\n\t"
                "v_cvt_pk_bf16_f32 v57, v58, v59\n\t"
                "v_cvt_pk_bf16_f32 v58, v72, v73\n\t"
                "v_cvt_pk_bf16_f32 v59, v74, v75\n\t"
                "ds_write_b128 %[a23], v[56:59] offset:0\n\t"
                "v_pk_fma_f32 v[28:29], v[96:97], v[18:19], v[28:29] op_sel:[0,1,0] op_sel_hi:[1,1,1]\n\t"
                "v_pk_fma_f32 v[30:31], v[98:99], v[18:19], v[30:31] op_sel:[0,1,0] op_sel_hi:[1,1,1]\n\t"
                "v_pk_fma_f32 v[44:45], v[100:101], v[18:19], v[44:45] op_sel:[0,1,0] op_sel_hi:[1,1,1]\n\t"
                "v_pk_fma_f32 v[46:47], v[102:103], v[18:19], v[46:47] op_sel:[0,1,0] op_sel_hi:[1,1,1]\n\t"
                "v_pk_mul_f32 v[28:29], v[104:105], v[28:29]\n\t"
                "v_pk_mul_f32 v[30:31], v[106:107], v[30:31]\n\t"
                "v_pk_mul_f32 v[44:45], v[108:109], v[44:45]\n\t"
                "v_pk_mul_f32 v[46:47], v[110:111], v[46:47]\n\t"
                "v_cvt_pk_bf16_f32 v28, v28, v29\n\t"
                "v_cvt_pk_bf16_f32 v29, v30, v31\n\t"
                "v_cvt_pk_bf16_f32 v30, v44, v45\n\t"
                "v_cvt_pk_bf16_f32 v31, v46, v47\n\t"
                "ds_write_b128 %[a01], v[28:31] offset:1024\n\t"
                "v_pk_fma_f32 v[60:61], v[128:129], v[18:19], v[60:61] op_sel:[0,1,0] op_sel_hi:[1,1,1]\n\t"
                "v_pk_fma_f32 v[62:63], v[130:131], v[18:19], v[62:63] op_sel:[0,1,0] op_sel_hi:[1,1,1]\n\t"
                "v_pk_fma_f32 v[76:77], v[132:133], v[18:19], v[76:77] op_sel:[0,1,0] op_sel_hi:[1,1,1]\n\t"
                "v_pk_fma_f32 v[78:79], v[134:135], v[18:19], v[78:79] op_sel:[0,1,0] op_sel_hi:[1,1,1]\n\t"
                "v_pk_mul_f32 v[60:61], v[136:137], v[60:61]\n\t"
                "v_pk_mul_f32 v[62:63], v[138:139], v[62:63]\n\t"
                "v_pk_mul_f32 v[76:77], v[140:141], v[76:77]\n\t"
                "v_pk_mul_f32 v[78:79], v[142:143], v[78:79]\n\t"
                "v_cvt_pk_bf16_f32 v60, v60, v61\n\t"
                "v_cvt_pk_bf16_f32 v61, v62, v63\n\t"
                "v_cvt_pk_bf16_f32 v62, v76, v77\n\t"
                "v_cvt_pk_bf16_f32 v63, v78, v79\n\t"
                "ds_write_b128 %[a23], v[60:63] offset:1024\n\t"
                "v_pk_fma_f32 v[32:33], v[96:97], v[20:21], v[32:33] op_sel_hi:[1,0,1]\n\t"
                "v_pk_fma_f32 v[34:35], v[98:99], v[20:21], v[34:35] op_sel_hi:[1,0,1]\n\t"
                "v_pk_fma_f32 v[48:49], v[100:101], v[20:21], v[48:49] op_sel_hi:[1,0,1]\n\t"
                "v_pk_fma_f32 v[50:51], v[102:103], v[20:21], v[50:51] op_sel_hi:[1,0,1]\n\t"
                "v_pk_mul_f32 v[32:33], v[104:105], v[32:33]\n\t"
                "v_pk_mul_f32 v[34:35], v[106:107], v[34:35]\n\t"
                "v_pk_mul_f32 v[48:49], v[108:109], v[48:49]\n\t"
                "v_pk_mul_f32 v[50:51], v[110:111], v[50:51]\n\t"
                "v_cvt_pk_bf16_f32 v32, v32, v33\n\t"
                "v_cvt_pk_bf16_f32 v33, v34, v35\n\t"
                "v_cvt_pk_bf16_f32 v34, v48, v49\n\t"
                "v_cvt_pk_bf16_f32 v35, v50, v51\n\t"
                "ds_write_b128 %[a01], v[32:35] offset:2048\n\t"
                "v_pk_fma_f32 v[64:65], v[128:129], v[20:21], v[64:65] op_sel_hi:[1,0,1]\n\t"
                "v_pk_fma_f32 v[66:67], v[130:131], v[20:21], v[66:67] op_sel_hi:[1,0,1]\n\t"
                "v_pk_fma_f32 v[80:81], v[132:133], v[20:21], v[80:81] op_sel_hi:[1,0,1]\n\t"
                "v_pk_fma_f32 v[82:83], v[134:135], v[20:21], v[82:83] op_sel_hi:[1,0,1]\n\t"
                "v_pk_mul_f32 v[64:65], v[136:137], v[64:65]\n\t"
                "v_pk_mul_f32 v[66:67], v[138:139], v[66:67]\n\t"
                "v_pk_mul_f32 v[80:81], v[140:141], v[80:81]\n\t"
                "v_pk_mul_f32 v[82:83], v[142:143], v[82:83]\n\t"
                "v_cvt_pk_bf16_f32 v64, v64, v65\n\t"
                "v_cvt_pk_bf16_f32 v65, v66, v67\n\t"
                "v_cvt_pk_bf16_f32 v66, v80, v81\n\t"
                "v_cvt_pk_bf16_f32 v67, v82, v83\n\t"
                "ds_write_b128 %[a23], v[64:67] offset:2048\n\t"
                "v_pk_fma_f32 v[36:37], v[96:97], v[20:21], v[36:37] op_sel:[0,1,0] op_sel_hi:[1,1,1]\n\t"
                "v_pk_fma_f32 v[38:39], v[98:99], v[20:21], v[38:39] op_sel:[0,1,0] op_sel_hi:[1,1,1]\n\t"
                "v_pk_fma_f32 v[52:53], v[100:101], v[20:21], v[52:53] op_sel:[0,1,0] op_sel_hi:[1,1,1]\n\t"
                "v_pk_fma_f32 v[54:55], v[102:103], v[20:21], v[54:55] op_sel:[0,1,0] op_sel_hi:[1,1,1]\n\t"
                "v_pk_mul_f32 v[36:37], v[104:105], v[36:37]\n\t"
                "v_pk_mul_f32 v[38:39], v[106:107], v[38:39]\n\t"
                "v_pk_mul_f32 v[52:53], v[108:109], v[52:53]\n\t"
                "v_pk_mul_f32 v[54:55], v[110:111], v[54:55]\n\t"
                "v_cvt_pk_bf16_f32 v36, v36, v37\n\t"
                "v_cvt_pk_bf16_f32 v37, v38, v39\n\t"
                "v_cvt_pk_bf16_f32 v38, v52, v53\n\t"
                "v_cvt_pk_bf16_f32 v39, v54, v55\n\t"
                "ds_write_b128 %[a01], v[36:39] offset:3072\n\t"
                "v_pk_fma_f32 v[68:69], v[128:129], v[20:21], v[68:69] op_sel:[0,1,0] op_sel_hi:[1,1,1]\n\t"
                "v_pk_fma_f32 v[70:71], v[130:131], v[20:21], v[70:71] op_sel:[0,1,0] op_sel_hi:[1,1,1]\n\t"
                "v_pk_fma_f32 v[84:85], v[132:133], v[20:21], v[84:85] op_sel:[0,1,0] op_sel_hi:[1,1,1]\n\t"
                "v_pk_fma_f32 v[86:87], v[134:135], v[20:21], v[86:87] op_sel:[0,1,0] op_sel_hi:[1,1,1]\n\t"
                "v_pk_mul_f32 v[68:69], v[136:137], v[68:69]\n\t"
                "v_pk_mul_f32 v[70:71], v[138:139], v[70:71]\n\t"
                "v_pk_mul_f32 v[84:85], v[140:141], v[84:85]\n\t"
                "v_pk_mul_f32 v[86:87], v[142:143], v[86:87]\n\t"
                "v_cvt_pk_bf16_f32 v68, v68, v69\n\t"
                "v_cvt_pk_bf16_f32 v69, v70, v71\n\t"
                "v_cvt_pk_bf16_f32 v70, v84, v85\n\t"
                "v_cvt_pk_bf16_f32 v71, v86, v87\n\t"
                "ds_write_b128 %[a23], v[68:71] offset:3072\n\t"
                "v_pk_fma_f32 v[88:89], v[96:97], v[22:23], v[88:89] op_sel_hi:[1,0,1]\n\t"
                "v_pk_fma_f32 v[90:91], v[98:99], v[22:23], v[90:91] op_sel_hi:[1,0,1]\n\t"
                "v_pk_fma_f32 v[92:93], v[100:101], v[22:23], v[92:93] op_sel_hi:[1,0,1]\n\t"
                "v_pk_fma_f32 v[94:95], v[102:103], v[22:23], v[94:95] op_sel_hi:[1,0,1]\n\t"
                "v_pk_mul_f32 v[88:89], v[104:105], v[88:89]\n\t"
                "v_pk_mul_f32 v[90:91], v[106:107], v[90:91]\n\t"
                "v_pk_mul_f32 v[92:93], v[108:109], v[92:93]\n\t"
                "v_pk_mul_f32 v[94:95], v[110:111], v[94:95]\n\t"
                "v_cvt_pk_bf16_f32 v88, v88, v89\n\t"
                "v_cvt_pk_bf16_f32 v89, v90, v91\n\t"
                "v_cvt_pk_bf16_f32 v90, v92, v93\n\t"
                "v_cvt_pk_bf16_f32 v91, v94, v95\n\t"
                "ds_write_b128 %[b0], v[88:91] offset:0\n\t"
                "s_branch LE%=\n\t"
                "LG%=:\n\t"
                "v_mfma_f32_16x16x32_bf16 v[84:87], v[108:111], v[140:143], v[84:87]\n\t"
                "v_lshrrev_b32 %[a01], 3, %[l16]\n\t"
                "v_and_b32 %[a01], 0x60, %[a01]\n\t"
                "v_add_u32 %[a01], %[sSt], %[a01]\n\t"
                "v_mfma_f32_16x16x32_bf16 v[88:91], v[96:99], v[144:147], v[88:91]\n\t"
                "v_xor_b32 %[a23], 0x80, %[a01]\n\t"
                "v_lshrrev_b32 %[b8], 2, %[l16]\n\t"
                "v_and_b32 %[b8], 60, %[b8]\n\t"
                "v_mfma_f32_16x16x32_bf16 v[92:95], v[100:103], v[144:147], v[92:95]\n\t"
                "v_add_u32 %[b0], %[sCs], %[b8]\n\t"
                "s_waitcnt lgkmcnt(0)\n\t"
                "s_barrier\n\t"
                "v_or_b32 %[b8], 0x20000, %[b8]\n\t"
                "v_mfma_f32_16x16x32_bf16 v[24:27], v[112:115], v[148:151], v[24:27]\n\t"
                "ds_read2_b32 v[18:19], %[b0] offset0:0 offset1:16\n\t"
                "ds_read_b128 v[96:99], %[a01] offset:0\n\t"
                "v_mfma_f32_16x16x32_bf16 v[40:43], v[116:119], v[148:151], v[40:43]\n\t"
                "ds_read_b128 v[100:103], %[a01] offset:16\n\t"
                "ds_read_b128 v[104:107], %[a01] offset:1024\n\t"
                "v_mfma_f32_16x16x32_bf16 v[56:59], v[120:123], v[148:151], v[56:59]\n\t"
                "ds_read_b128 v[108:111], %[a01] offset:1040\n\t"
                "ds_read_b128 v[128:131], %[a23] offset:0\n\t"
                "v_mfma_f32_16x16x32_bf16 v[72:75], v[124:127], v[148:151], v[72:75]\n\t"
                "ds_read_b128 v[132:135], %[a23] offset:16\n\t"
                "ds_read_b128 v[136:139], %[a23] offset:1024\n\t"
                "v_mfma_f32_16x16x32_bf16 v[28:31], v[112:115], v[152:155], v[28:31]\n\t"
                "ds_read_b128 v[140:143], %[a23] offset:1040\n\t"
                "ds_read2_b32 v[20:21], %[b0] offset0:32 offset1:48\n\t"
                "v_mfma_f32_16x16x32_bf16 v[44:47], v[116:119], v[152:155], v[44:47]\n\t"
                "ds_read_b32 v22, %[b8] offset:31232\n\t"
                "s_waitcnt lgkmcnt(0)\n\t"
                "v_mfma_f32_16x16x32_bf16 v[60:63], v[120:123], v[152:155], v[60:63]\n\t"
                "v_fmac_f32 v24, v96, v18\n\t"
                "v_fmac_f32 v25, v97, v18\n\t"
                "v_fmac_f32 v26, v98, v18\n\t"
                "v_mfma_f32_16x16x32_bf16 v[76:79], v[124:127], v[152:155], v[76:79]\n\t"
                "v_fmac_f32 v27, v99, v18\n\t"
                "v_fmac_f32 v40, v100, v18\n\t"
                "v_fmac_f32 v41, v101, v18\n\t"
                "v_mfma_f32_16x16x32_bf16 v[32:35], v[112:115], v[156:159], v[32:35]\n\t"
                "v_fmac_f32 v42, v102, v18\n\t"
                "v_fmac_f32 v43, v103, v18\n\t"
                "v_mul_f32 v24, v104, v24\n\t"
                "v_mfma_f32_16x16x32_bf16 v[48:51], v[116:119], v[156:159], v[48:51]\n\t"
                "v_mul_f32 v25, v105, v25\n\t"
                "v_mul_f32 v26, v106, v26\n\t"
                "v_mul_f32 v27, v107, v27\n\t"
                "v_mfma_f32_16x16x32_bf16 v[64:67], v[120:123], v[156:159], v[64:67]\n\t"
                "v_mul_f32 v40, v108, v40\n\t"
                "v_mul_f32 v41, v109, v41\n\t"
                "v_mul_f32 v42, v110, v42\n\t"
                "v_mfma_f32_16x16x32_bf16 v[80:83], v[124:127], v[156:159], v[80:83]\n\t"
                "v_mul_f32 v43, v111, v43\n\t"
                "v_cvt_pk_bf16_f32 v24, v24, v25\n\t"
                "v_cvt_pk_bf16_f32 v25, v26, v27\n\t"
                "v_mfma_f32_16x16x32_bf16 v[36:39], v[112:115], v[160:163], v[36:39]\n\t"
                "v_cvt_pk_bf16_f32 v26, v40, v41\n\t"
                "v_cvt_pk_bf16_f32 v27, v42, v43\n\t"
                "buffer_store_dwordx4 v[24:27], %[l16], %[rs], %[o0] offen offset:0 sc1\n\t"
                "v_fmac_f32 v56, v128, v18\n\t"
                "v_mfma_f32_16x16x32_bf16 v[52:55], v[116:119], v[160:163], v[52:55]\n\t"
                "v_fmac_f32 v57, v129, v18\n\t"
                "v_fmac_f32 v58, v130, v18\n\t"
                "v_fmac_f32 v59, v131, v18\n\t"
                "v_mfma_f32_16x16x32_bf16 v[68:71], v[120:123], v[160:163], v[68:71]\n\t"
                "v_fmac_f32 v72, v132, v18\n\t"
                "v_fmac_f32 v73, v133, v18\n\t"
                "v_fmac_f32 v74, v134, v18\n\t"
                "v_mfma_f32_16x16x32_bf16 v[84:87], v[124:127], v[160:163], v[84:87]\n\t"
                "v_fmac_f32 v75, v135, v18\n\t"
                "v_mul_f32 v56, v136, v56\n\t"
                "v_mul_f32 v57, v137, v57\n\t"
                "v_mfma_f32_16x16x32_bf16 v[88:91], v[112:115], v[164:167], v[88:91]\n\t"
                "v_mul_f32 v58, v138, v58\n\t"
                "v_mul_f32 v59, v139, v59\n\t"
                "v_mul_f32 v72, v140, v72\n\t"
                "v_mfma_f32_16x16x32_bf16 v[92:95], v[116:119], v[164:167], v[92:95]\n\t"
                "v_mul_f32 v73, v141, v73\n\t"
                "v_mul_f32 v74, v142, v74\n\t"
                "v_mul_f32 v75, v143, v75\n\t"
                "v_cvt_pk_bf16_f32 v56, v56, v57\n\t"
                "v_cvt_pk_bf16_f32 v57, v58, v59\n\t"
                "v_cvt_pk_bf16_f32 v58, v72, v73\n\t"
                "v_cvt_pk_bf16_f32 v59, v74, v75\n\t"
                "buffer_store_dwordx4 v[56:59], %[l16], %[rs], %[o1] offen offset:0 sc1\n\t"
                "v_pk_fma_f32 v[28:29], v[96:97], v[18:19], v[28:29] op_sel:[0,1,0] op_sel_hi:[1,1,1]\n\t"
                "v_pk_fma_f32 v[30:31], v[98:99], v[18:19], v[30:31] op_sel:[0,1,0] op_sel_hi:[1,1,1]\n\t"
                "v_pk_fma_f32 v[44:45], v[100:101], v[18:19], v[44:45] op_sel:[0,1,0] op_sel_hi:[1,1,1]\n\t"
                "v_pk_fma_f32 v[46:47], v[102:103], v[18:19], v[46:47] op_sel:[0,1,0] op_sel_hi:[1,1,1]\n\t"
                "v_pk_mul_f32 v[28:29], v[104:105], v[28:29]\n\t"
                "v_pk_mul_f32 v[30:31], v[106:107], v[30:31]\n\t"
                "v_pk_mul_f32 v[44:45], v[108:109], v[44:45]\n\t"
                "v_pk_mul_f32 v[46:47], v[110:111], v[46:47]\n\t"
                "v_cvt_pk_bf16_f32 v28, v28, v29\n\t"
                "v_cvt_pk_bf16_f32 v29, v30, v31\n\t"
                "v_cvt_pk_bf16_f32 v30, v44, v45\n\t"
                "v_cvt_pk_bf16_f32 v31, v46, v47\n\t"
                "buffer_store_dwordx4 v[28:31], %[l16], %[rs], %[o0] offen offset:1024 sc1\n\t"
                "v_pk_fma_f32 v[60:61], v[128:129], v[18:19], v[60:61] op_sel:[0,1,0] op_sel_hi:[1,1,1]\n\t"
                "v_pk_fma_f32 v[62:63], v[130:131], v[18:19], v[62:63] op_sel:[0,1,0] op_sel_hi:[1,1,1]\n\t"
                "v_pk_fma_f32 v[76:77], v[132:133], v[18:19], v[76:77] op_sel:[0,1,0] op_sel_hi:[1,1,1]\n\t"
                "v_pk_fma_f32 v[78:79], v[134:135], v[18:19], v[78:79] op_sel:[0,1,0] op_sel_hi:[1,1,1]\n\t"
                "v_pk_mul_f32 v[60:61], v[136:137], v[60:61]\n\t"
                "v_pk_mul_f32 v[62:63], v[138:139], v[62:63]\n\t"
                "v_pk_mul_f32 v[76:77], v[140:141], v[76:77]\n\t"
                "v_pk_mul_f32 v[78:79], v[142:143], v[78:79]\n\t"
                "v_cvt_pk_bf16_f32 v60, v60, v61\n\t"
                "v_cvt_pk_bf16_f32 v61, v62, v63\n\t"
                "v_cvt_pk_bf16_f32 v62, v76, v77\n\t"
                "v_cvt_pk_bf16_f32 v63, v78, v79\n\t"
                "buffer_store_dwordx4 v[60:63], %[l16], %[rs], %[o1] offen offset:1024 sc1\n\t"
                "v_pk_fma_f32 v[32:33], v[96:97], v[20:21], v[32:33] op_sel_hi:[1,0,1]\n\t"
                "v_pk_fma_f32 v[34:35], v[98:99], v[20:21], v[34:35] op_sel_hi:[1,0,1]\n\t"
                "v_pk_fma_f32 v[48:49], v[100:101], v[20:21], v[48:49] op_sel_hi:[1,0,1]\n\t"
                "v_pk_fma_f32 v[50:51], v[102:103], v[20:21], v[50:51] op_sel_hi:[1,0,1]\n\t"
                "v_pk_mul_f32 v[32:33], v[104:105], v[32:33]\n\t"
                "v_pk_mul_f32 v[34:35], v[106:107], v[34:35]\n\t"
                "v_pk_mul_f32 v[48:49], v[108:109], v[48:49]\n\t"
                "v_pk_mul_f32 v[50:51], v[110:111], v[50:51]\n\t"
                "v_cvt_pk_bf16_f32 v32, v32, v33\n\t"
                "v_cvt_pk_bf16_f32 v33, v34, v35\n\t"
                "v_cvt_pk_bf16_f32 v34, v48, v49\n\t"
                "v_cvt_pk_bf16_f32 v35, v50, v51\n\t"
                "buffer_store_dwordx4 v[32:35], %[l16], %[rs], %[o0] offen offset:2048 sc1\n\t"
                "v_pk_fma_f32 v[64:65], v[128:129], v[20:21], v[64:65] op_sel_hi:[1,0,1]\n\t"
                "v_pk_fma_f32 v[66:67], v[130:131], v[20:21], v[66:67] op_sel_hi:[1,0,1]\n\t"
                "v_pk_fma_f32 v[80:81], v[132:133], v[20:21], v[80:81] op_sel_hi:[1,0,1]\n\t"
                "v_pk_fma_f32 v[82:83], v[134:135], v[20:21], v[82:83] op_sel_hi:[1,0,1]\n\t"
                "v_pk_mul_f32 v[64:65], v[136:137], v[64:65]\n\t"
                "v_pk_mul_f32 v[66:67], v[138:139], v[66:67]\n\t"
                "v_pk_mul_f32 v[80:81], v[140:141], v[80:81]\n\t"
                "v_pk_mul_f32 v[82:83], v[142:143], v[82:83]\n\t"
                "v_cvt_pk_bf16_f32 v64, v64, v65\n\t"
                "v_cvt_pk_bf16_f32 v65, v66, v67\n\t"
                "v_cvt_pk_bf16_f32 v66, v80, v81\n\t"
                "v_cvt_pk_bf16_f32 v67, v82, v83\n\t"
                "buffer_store_dwordx4 v[64:67], %[l16], %[rs], %[o1] offen offset:2048 sc1\n\t"
                "v_pk_fma_f32 v[36:37], v[96:97], v[20:21], v[36:37] op_sel:[0,1,0] op_sel_hi:[1,1,1]\n\t"
                "v_pk_fma_f32 v[38:39], v[98:99], v[20:21], v[38:39] op_sel:[0,1,0] op_sel_hi:[1,1,1]\n\t"
                "v_pk_fma_f32 v[52:53], v[100:101], v[20:21], v[52:53] op_sel:[0,1,0] op_sel_hi:[1,1,1]\n\t"
                "v_pk_fma_f32 v[54:55], v[102:103], v[20:21], v[54:55] op_sel:[0,1,0] op_sel_hi:[1,1,1]\n\t"
                "v_pk_mul_f32 v[36:37], v[104:105], v[36:37]\n\t"
                "v_pk_mul_f32 v[38:39], v[106:107], v[38:39]\n\t"
                "v_pk_mul_f32 v[52:53], v[108:109], v[52:53]\n\t"
                "v_pk_mul_f32 v[54:55], v[110:111], v[54:55]\n\t"
                "v_cvt_pk_bf16_f32 v36, v36, v37\n\t"
                "v_cvt_pk_bf16_f32 v37, v38, v39\n\t"
                "v_cvt_pk_bf16_f32 v38, v52, v53\n\t"
                "v_cvt_pk_bf16_f32 v39, v54, v55\n\t"
                "buffer_store_dwordx4 v[36:39], %[l16], %[rs], %[o0] offen offset:3072 sc1\n\t"
                "v_pk_fma_f32 v[68:69], v[128:129], v[20:21], v[68:69] op_sel:[0,1,0] op_sel_hi:[1,1,1]\n\t"
                "v_pk_fma_f32 v[70:71], v[130:131], v[20:21], v[70:71] op_sel:[0,1,0] op_sel_hi:[1,1,1]\n\t"
                "v_pk_fma_f32 v[84:85], v[132:133], v[20:21], v[84:85] op_sel:[0,1,0] op_sel_hi:[1,1,1]\n\t"
                "v_pk_fma_f32 v[86:87], v[134:135], v[20:21], v[86:87] op_sel:[0,1,0] op_sel_hi:[1,1,1]\n\t"
                "v_pk_mul_f32 v[68:69], v[136:137], v[68:69]\n\t"
                "v_pk_mul_f32 v[70:71], v[138:139], v[70:71]\n\t"
                "v_pk_mul_f32 v[84:85], v[140:141], v[84:85]\n\t"
                "v_pk_mul_f32 v[86:87], v[142:143], v[86:87]\n\t"
                "v_cvt_pk_bf16_f32 v68, v68, v69\n\t"
                "v_cvt_pk_bf16_f32 v69, v70, v71\n\t"
                "v_cvt_pk_bf16_f32 v70, v84, v85\n\t"
                "v_cvt_pk_bf16_f32 v71, v86, v87\n\t"
                "buffer_store_dwordx4 v[68:71], %[l16], %[rs], %[o1] offen offset:3072 sc1\n\t"
                "v_pk_fma_f32 v[88:89], v[96:97], v[22:23], v[88:89] op_sel_hi:[1,0,1]\n\t"
                "v_pk_fma_f32 v[90:91], v[98:99], v[22:23], v[90:91] op_sel_hi:[1,0,1]\n\t"
                "v_pk_fma_f32 v[92:93], v[100:101], v[22:23], v[92:93] op_sel_hi:[1,0,1]\n\t"
                "v_pk_fma_f32 v[94:95], v[102:103], v[22:23], v[94:95] op_sel_hi:[1,0,1]\n\t"
                "v_pk_mul_f32 v[88:89], v[104:105], v[88:89]\n\t"
                "v_pk_mul_f32 v[90:91], v[106:107], v[90:91]\n\t"
                "v_pk_mul_f32 v[92:93], v[108:109], v[92:93]\n\t"
                "v_pk_mul_f32 v[94:95], v[110:111], v[94:95]\n\t"
                "v_cvt_pk_bf16_f32 v88, v88, v89\n\t"
                "v_cvt_pk_bf16_f32 v89, v90, v91\n\t"
                "v_cvt_pk_bf16_f32 v90, v92, v93\n\t"
                "v_cvt_pk_bf16_f32 v91, v94, v95\n\t"
                "buffer_store_dwordx4 v[88:91], %[l16], %[rs], %[o8] offen offset:0 sc1\n\t"
                "s_nop 1\n\t"
                "LE%=:\n\t"
                : [a01] "=&v"(a01), [a23] "=&v"(a23), [b0] "=&v"(b0), [b8] "=&v"(b8), [sA] "+&s"(sA), [sB] "+&s"(sB), [sS] "+&s"(sS), [sN] "+&s"(sN)
                : [rowl] "v"(rowl), [l16] "v"(l16), [wA] "s"(wA), [sD] "s"(sD), [sSt] "s"(sSt), [sCs] "s"(sCs), [sG] "s"(wm), [o0] "s"(o0), [o1] "s"(o1), [o8] "s"(o8), [rs] "s"(rs_ws)
                : "memory", "scc", "v18", "v19", "v20", "v21", "v22", "v23", "v24", "v25", "v26", "v27", "v28", "v29", "v30", "v31", "v32", "v33", "v34", "v35", "v36", "v37", "v38", "v39", "v40", "v41", "v42", "v43", "v44", "v45", "v46", "v47", "v48", "v49", "v50", "v51", "v52", "v53", "v54", "v55", "v56", "v57", "v58", "v59", "v60", "v61", "v62", "v63", "v64", "v65", "v66", "v67", "v68", "v69", "v70", "v71", "v72", "v73", "v74", "v75", "v76", "v77", "v78", "v79", "v80", "v81", "v82", "v83", "v84", "v85", "v86", "v87", "v88", "v89", "v90", "v91", "v92", "v93", "v94", "v95", "v96", "v97", "v98", "v99", "v100", "v101", "v102", "v103", "v104", "v105", "v106", "v107", "v108", "v109", "v110", "v111", "v112", "v113", "v114", "v115", "v116", "v117", "v118", "v119", "v120", "v121", "v122", "v123", "v124", "v125", "v126", "v127", "v128", "v129", "v130", "v131", "v132", "v133", "v134", "v135", "v136", "v137", "v138", "v139", "v140", "v141", "v142", "v143", "v144", "v145", "v146", "v147", "v148", "v149", "v150", "v151", "v152", "v153", "v154", "v155", "v156", "v157", "v158", "v159", "v160", "v161", "v162", "v163", "v164", "v165", "v166", "v167");
            PIN(lv);
            STAMP(2);
            if (wm == 3) {
                tag = lds_ld<u32>(L_TAG);
                WAIT_V(0);
                if (lv < 16 * FW) st_flag(ws + WS_FAB + ((j * 8 + mb) * 4 + 3) * FS + wn * HS + (lv >> 2), tag);
            }
        }
        {
            const u32 l16 = lv, l = lv >> 4, q = l >> 4, r16 = l & 15;
            u32 wo = w;
            asm volatile("" : "+s"(wo));
            const u32 wm = wo & 3u, wn = wo >> 2;
            const u32 trl = ((l >> 2) & 7) * 64u + 32u * (q & 1) + 8u * (l & 3), rowl = r16 * 128u + 16u * (q ^ (r16 >> 1));
            const u32 wA = (u32)wm * 8192u + (u32)wn * 4096u, wB = L_B + (u32)wn * 4096u;
            const u32 trb = (u32)(l >> 5) * 2048u + trl, trs = (u32)(l >> 5) * 512u + (u32)(l & 31) * 8u;
            u32 a01, a23, b0, b1, b8, st;
            u32 sA = wA, sS = L_B + 8192u + 36864u, sB = wB + 36864u, sT = (u32)(-c0);
            const u32 sD = (u32)wn * 4096u - 8192u, sE = 16u - c0, sK = 8, sO = wo ^ (u32)j;
            const u32 pd = (sO && !XP) ? WS_P + (u32)((g * 8 + wo) * 72 + j) * 1024u + l16 : 0x80000000u;
            WAIT_L(0); BAR(); SB();
            STAMP(3);
            asm volatile(
                "s_cmp_lt_u32 %[sT], %[sK]\n\t"
                "s_cbranch_scc1 LPC%=\n\t"
                "v_add_u32 %[a01], %[sA], %[l16]\n\t"
                "v_xor_b32 %[a23], 0x1000, %[a01]\n\t"
                "v_add_u32 %[b0], %[sB], %[trb]\n\t"
                "v_xor_b32 %[b1], 32, %[b0]\n\t"
                "v_add_u32 %[b8], %[sS], %[trs]\n\t"
                "ds_read_b128 v[96:99], %[a01] offset:0\n\t"
                "ds_read_b128 v[100:103], %[a01] offset:2048\n\t"
                "ds_read_b128 v[104:107], %[a23] offset:0\n\t"
                "ds_read_b128 v[108:111], %[a23] offset:2048\n\t"
                "ds_read_b64_tr_b16 v[128:129], %[b0] offset:0\n\t"
                "ds_read_b64_tr_b16 v[130:131], %[b0] offset:1024\n\t"
                "ds_read_b64_tr_b16 v[132:133], %[b1] offset:0\n\t"
                "ds_read_b64_tr_b16 v[134:135], %[b1] offset:1024\n\t"
                "ds_read_b64_tr_b16 v[136:137], %[b0] offset:512\n\t"
                "ds_read_b64_tr_b16 v[138:139], %[b0] offset:1536\n\t"
                "ds_read_b64_tr_b16 v[140:141], %[b1] offset:512\n\t"
                "ds_read_b64_tr_b16 v[142:143], %[b1] offset:1536\n\t"
                "ds_read_b64_tr_b16 v[144:145], %[b8] offset:0\n\t"
                "ds_read_b64_tr_b16 v[146:147], %[b8] offset:256\n\t"
                "s_waitcnt lgkmcnt(0)\n\t"
                "v_mfma_f32_16x16x32_bf16 v[24:27], v[128:131], v[96:99], 0\n\t"
                "ds_read_b128 v[112:115], %[a01] offset:1024\n\t"
                "v_mfma_f32_16x16x32_bf16 v[40:43], v[128:131], v[100:103], 0\n\t"
                "ds_read_b128 v[116:119], %[a01] offset:3072\n\t"
                "v_mfma_f32_16x16x32_bf16 v[56:59], v[128:131], v[104:107], 0\n\t"
                "ds_read_b128 v[120:123], %[a23] offset:1024\n\t"
                "v_mfma_f32_16x16x32_bf16 v[72:75], v[128:131], v[108:111], 0\n\t"
                "ds_read_b128 v[124:127], %[a23] offset:3072\n\t"
                "v_mfma_f32_16x16x32_bf16 v[28:31], v[132:135], v[96:99], 0\n\t"
                "ds_read_b64_tr_b16 v[148:149], %[b0] offset:9216\n\t"
                "ds_read_b64_tr_b16 v[150:151], %[b0] offset:10240\n\t"
                "v_mfma_f32_16x16x32_bf16 v[44:47], v[132:135], v[100:103], 0\n\t"
                "ds_read_b64_tr_b16 v[152:153], %[b1] offset:9216\n\t"
                "ds_read_b64_tr_b16 v[154:155], %[b1] offset:10240\n\t"
                "v_mfma_f32_16x16x32_bf16 v[60:63], v[132:135], v[104:107], 0\n\t"
                "ds_read_b64_tr_b16 v[156:157], %[b0] offset:9728\n\t"
                "ds_read_b64_tr_b16 v[158:159], %[b0] offset:10752\n\t"
                "v_mfma_f32_16x16x32_bf16 v[76:79], v[132:135], v[108:111], 0\n\t"
                "ds_read_b64_tr_b16 v[160:161], %[b1] offset:9728\n\t"
                "ds_read_b64_tr_b16 v[162:163], %[b1] offset:10752\n\t"
                "v_mfma_f32_16x16x32_bf16 v[32:35], v[136:139], v[96:99], 0\n\t"
                "ds_read_b64_tr_b16 v[164:165], %[b8] offset:9216\n\t"
                "ds_read_b64_tr_b16 v[166:167], %[b8] offset:9472\n\t"
                "v_mfma_f32_16x16x32_bf16 v[48:51], v[136:139], v[100:103], 0\n\t"
                "s_add_u32 %[sA], %[sA], 0x8000\n\t"
                "s_cmp_lt_u32 %[sA], 0x18000\n\t"
                "s_cselect_b32 %[sA], %[sA], %[wA]\n\t"
                "v_mfma_f32_16x16x32_bf16 v[64:67], v[136:139], v[104:107], 0\n\t"
                "s_add_u32 %[sS], %[sS], 18432\n\t"
                "s_cmp_lt_u32 %[sS], 161792\n\t"
                "s_cselect_b32 %[sS], %[sS], 106496\n\t"
                "v_mfma_f32_16x16x32_bf16 v[80:83], v[136:139], v[108:111], 0\n\t"
                "s_add_u32 %[sB], %[sS], %[sD]\n\t"
                "s_add_u32 %[sT], %[sT], 1\n\t"
                "v_mfma_f32_16x16x32_bf16 v[36:39], v[140:143], v[96:99], 0\n\t"
                "s_cmp_lt_u32 %[sT], %[sK]\n\t"
                "v_mfma_f32_16x16x32_bf16 v[52:55], v[140:143], v[100:103], 0\n\t"
                "v_mfma_f32_16x16x32_bf16 v[68:71], v[140:143], v[104:107], 0\n\t"
                "v_mfma_f32_16x16x32_bf16 v[84:87], v[140:143], v[108:111], 0\n\t"
                "v_mfma_f32_16x16x32_bf16 v[88:91], v[144:147], v[96:99], 0\n\t"
                "v_mfma_f32_16x16x32_bf16 v[92:95], v[144:147], v[100:103], 0\n\t"
                "s_cbranch_scc1 LCW%=\n\t"
                "s_branch LHW%=\n\t"
                "LPC%=:\n\t"
                "v_add_u32 %[a01], %[sA], %[rowl]\n\t"
                "v_xor_b32 %[a23], 0x1000, %[a01]\n\t"
                "v_add_u32 %[b0], %[sB], %[l16]\n\t"
                "v_add_u32 %[b8], %[sS], %[l16]\n\t"
                "ds_read_b128 v[96:99], %[a01] offset:0\n\t"
                "ds_read_b128 v[100:103], %[a01] offset:2048\n\t"
                "ds_read_b128 v[104:107], %[a23] offset:0\n\t"
                "ds_read_b128 v[108:111], %[a23] offset:2048\n\t"
                "ds_read_b128 v[128:131], %[b0] offset:0\n\t"
                "ds_read_b128 v[132:135], %[b0] offset:1024\n\t"
                "ds_read_b128 v[136:139], %[b0] offset:2048\n\t"
                "ds_read_b128 v[140:143], %[b0] offset:3072\n\t"
                "ds_read_b128 v[144:147], %[b8] offset:0\n\t"
                "s_waitcnt lgkmcnt(0)\n\t"
                "v_mfma_f32_16x16x32_bf16 v[24:27], v[128:131], v[96:99], 0\n\t"
                "v_xor_b32 %[a01], 64, %[a01]\n\t"
                "v_xor_b32 %[a23], 64, %[a23]\n\t"
                "ds_read_b128 v[112:115], %[a01] offset:0\n\t"
                "v_mfma_f32_16x16x32_bf16 v[40:43], v[128:131], v[100:103], 0\n\t"
                "ds_read_b128 v[116:119], %[a01] offset:2048\n\t"
                "v_mfma_f32_16x16x32_bf16 v[56:59], v[128:131], v[104:107], 0\n\t"
                "ds_read_b128 v[120:123], %[a23] offset:0\n\t"
                "v_mfma_f32_16x16x32_bf16 v[72:75], v[128:131], v[108:111], 0\n\t"
                "ds_read_b128 v[124:127], %[a23] offset:2048\n\t"
                "v_mfma_f32_16x16x32_bf16 v[28:31], v[132:135], v[96:99], 0\n\t"
                "ds_read_b128 v[148:151], %[b0] offset:9216\n\t"
                "v_mfma_f32_16x16x32_bf16 v[44:47], v[132:135], v[100:103], 0\n\t"
                "ds_read_b128 v[152:155], %[b0] offset:10240\n\t"
                "v_mfma_f32_16x16x32_bf16 v[60:63], v[132:135], v[104:107], 0\n\t"
                "ds_read_b128 v[156:159], %[b0] offset:11264\n\t"
                "v_mfma_f32_16x16x32_bf16 v[76:79], v[132:135], v[108:111], 0\n\t"
                "ds_read_b128 v[160:163], %[b0] offset:12288\n\t"
                "v_mfma_f32_16x16x32_bf16 v[32:35], v[136:139], v[96:99], 0\n\t"
                "ds_read_b128 v[164:167], %[b8] offset:9216\n\t"
                "v_mfma_f32_16x16x32_bf16 v[48:51], v[136:139], v[100:103], 0\n\t"
                "s_add_u32 %[sA], %[sA], 0x8000\n\t"
                "s_cmp_lt_u32 %[sA], 0x18000\n\t"
                "s_cselect_b32 %[sA], %[sA], %[wA]\n\t"
                "v_mfma_f32_16x16x32_bf16 v[64:67], v[136:139], v[104:107], 0\n\t"
                "s_add_u32 %[sS], %[sS], 18432\n\t"
                "s_cmp_lt_u32 %[sS], 161792\n\t"
                "s_cselect_b32 %[sS], %[sS], 106496\n\t"
                "v_mfma_f32_16x16x32_bf16 v[80:83], v[136:139], v[108:111], 0\n\t"
                "s_add_u32 %[sB], %[sS], %[sD]\n\t"
                "s_add_u32 %[sT], %[sT], 1\n\t"
                "v_mfma_f32_16x16x32_bf16 v[36:39], v[140:143], v[96:99], 0\n\t"
                "s_cmp_lt_u32 %[sT], %[sK]\n\t"
                "v_mfma_f32_16x16x32_bf16 v[52:55], v[140:143], v[100:103], 0\n\t"
                "v_mfma_f32_16x16x32_bf16 v[68:71], v[140:143], v[104:107], 0\n\t"
                "v_mfma_f32_16x16x32_bf16 v[84:87], v[140:143], v[108:111], 0\n\t"
                "v_mfma_f32_16x16x32_bf16 v[88:91], v[144:147], v[96:99], 0\n\t"
                "v_mfma_f32_16x16x32_bf16 v[92:95], v[144:147], v[100:103], 0\n\t"
                "s_cbranch_scc1 LCW%=\n\t"
                "s_branch LHW%=\n\t"
                ".p2align 6\n\t"
                "LC%=:\n\t"
                "v_mfma_f32_16x16x32_bf16 v[68:71], v[140:143], v[104:107], v[68:71]\n\t"
                "v_mfma_f32_16x16x32_bf16 v[84:87], v[140:143], v[108:111], v[84:87]\n\t"
                "v_mfma_f32_16x16x32_bf16 v[88:91], v[144:147], v[96:99], v[88:91]\n\t"
                "v_mfma_f32_16x16x32_bf16 v[92:95], v[144:147], v[100:103], v[92:95]\n\t"
                "LCW%=:\n\t"
                "s_waitcnt lgkmcnt(0)\n\t"
                "s_barrier\n\t"
                "v_mfma_f32_16x16x32_bf16 v[24:27], v[148:151], v[112:115], v[24:27]\n\t"
                "v_add_u32 %[a01], %[sA], %[rowl]\n\t"
                "v_xor_b32 %[a23], 0x1000, %[a01]\n\t"
                "ds_read_b128 v[96:99], %[a01] offset:0\n\t"
                "v_mfma_f32_16x16x32_bf16 v[40:43], v[148:151], v[116:119], v[40:43]\n\t"
                "v_add_u32 %[b0], %[sB], %[l16]\n\t"
                "v_add_u32 %[b8], %[sS], %[l16]\n\t"
                "ds_read_b128 v[100:103], %[a01] offset:2048\n\t"
                "v_mfma_f32_16x16x32_bf16 v[56:59], v[148:151], v[120:123], v[56:59]\n\t"
                "ds_read_b128 v[104:107], %[a23] offset:0\n\t"
                "v_mfma_f32_16x16x32_bf16 v[72:75], v[148:151], v[124:127], v[72:75]\n\t"
                "ds_read_b128 v[108:111], %[a23] offset:2048\n\t"
                "v_mfma_f32_16x16x32_bf16 v[28:31], v[152:155], v[112:115], v[28:31]\n\t"
                "ds_read_b128 v[128:131], %[b0] offset:0\n\t"
                "v_mfma_f32_16x16x32_bf16 v[44:47], v[152:155], v[116:119], v[44:47]\n\t"
                "ds_read_b128 v[132:135], %[b0] offset:1024\n\t"
                "v_mfma_f32_16x16x32_bf16 v[60:63], v[152:155], v[120:123], v[60:63]\n\t"
                "ds_read_b128 v[136:139], %[b0] offset:2048\n\t"
                "v_mfma_f32_16x16x32_bf16 v[76:79], v[152:155], v[124:127], v[76:79]\n\t"
                "ds_read_b128 v[140:143], %[b0] offset:3072\n\t"
                "v_mfma_f32_16x16x32_bf16 v[32:35], v[156:159], v[112:115], v[32:35]\n\t"
                "ds_read_b128 v[144:147], %[b8] offset:0\n\t"
                "v_mfma_f32_16x16x32_bf16 v[48:51], v[156:159], v[116:119], v[48:51]\n\t"
                "v_mfma_f32_16x16x32_bf16 v[64:67], v[156:159], v[120:123], v[64:67]\n\t"
                "v_mfma_f32_16x16x32_bf16 v[80:83], v[156:159], v[124:127], v[80:83]\n\t"
                "v_mfma_f32_16x16x32_bf16 v[36:39], v[160:163], v[112:115], v[36:39]\n\t"
                "v_mfma_f32_16x16x32_bf16 v[52:55], v[160:163], v[116:119], v[52:55]\n\t"
                "v_mfma_f32_16x16x32_bf16 v[68:71], v[160:163], v[120:123], v[68:71]\n\t"
                "v_mfma_f32_16x16x32_bf16 v[84:87], v[160:163], v[124:127], v[84:87]\n\t"
                "v_mfma_f32_16x16x32_bf16 v[88:91], v[164:167], v[112:115], v[88:91]\n\t"
                "v_mfma_f32_16x16x32_bf16 v[92:95], v[164:167], v[116:119], v[92:95]\n\t"
                "s_waitcnt lgkmcnt(0)\n\t"
                "v_mfma_f32_16x16x32_bf16 v[24:27], v[128:131], v[96:99], v[24:27]\n\t"
                "v_xor_b32 %[a01], 64, %[a01]\n\t"
                "v_xor_b32 %[a23], 64, %[a23]\n\t"
                "ds_read_b128 v[112:115], %[a01] offset:0\n\t"
                "v_mfma_f32_16x16x32_bf16 v[40:43], v[128:131], v[100:103], v[40:43]\n\t"
                "ds_read_b128 v[116:119], %[a01] offset:2048\n\t"
                "v_mfma_f32_16x16x32_bf16 v[56:59], v[128:131], v[104:107], v[56:59]\n\t"
                "ds_read_b128 v[120:123], %[a23] offset:0\n\t"
                "v_mfma_f32_16x16x32_bf16 v[72:75], v[128:131], v[108:111], v[72:75]\n\t"
                "ds_read_b128 v[124:127], %[a23] offset:2048\n\t"
                "v_mfma_f32_16x16x32_bf16 v[28:31], v[132:135], v[96:99], v[28:31]\n\t"
                "ds_read_b128 v[148:151], %[b0] offset:9216\n\t"
                "v_mfma_f32_16x16x32_bf16 v[44:47], v[132:135], v[100:103], v[44:47]\n\t"
                "ds_read_b128 v[152:155], %[b0] offset:10240\n\t"
                "v_mfma_f32_16x16x32_bf16 v[60:63], v[132:135], v[104:107], v[60:63]\n\t"
                "ds_read_b128 v[156:159], %[b0] offset:11264\n\t"
                "v_mfma_f32_16x16x32_bf16 v[76:79], v[132:135], v[108:111], v[76:79]\n\t"
                "ds_read_b128 v[160:163], %[b0] offset:12288\n\t"
                "v_mfma_f32_16x16x32_bf16 v[32:35], v[136:139], v[96:99], v[32:35]\n\t"
                "ds_read_b128 v[164:167], %[b8] offset:9216\n\t"
                "v_mfma_f32_16x16x32_bf16 v[48:51], v[136:139], v[100:103], v[48:51]\n\t"
                "s_add_u32 %[sA], %[sA], 0x8000\n\t"
                "s_cmp_lt_u32 %[sA], 0x18000\n\t"
                "s_cselect_b32 %[sA], %[sA], %[wA]\n\t"
                "v_mfma_f32_16x16x32_bf16 v[64:67], v[136:139], v[104:107], v[64:67]\n\t"
                "s_add_u32 %[sS], %[sS], 18432\n\t"
                "s_cmp_lt_u32 %[sS], 161792\n\t"
                "s_cselect_b32 %[sS], %[sS], 106496\n\t"
                "v_mfma_f32_16x16x32_bf16 v[80:83], v[136:139], v[108:111], v[80:83]\n\t"
                "s_add_u32 %[sB], %[sS], %[sD]\n\t"
                "s_add_u32 %[sT], %[sT], 1\n\t"
                "v_mfma_f32_16x16x32_bf16 v[36:39], v[140:143], v[96:99], v[36:39]\n\t"
                "s_cmp_lt_u32 %[sT], %[sK]\n\t"
                "v_mfma_f32_16x16x32_bf16 v[52:55], v[140:143], v[100:103], v[52:55]\n\t"
                "s_cbranch_scc1 LC%=\n\t"
                "LH%=:\n\t"
                "v_mfma_f32_16x16x32_bf16 v[68:71], v[140:143], v[104:107], v[68:71]\n\t"
                "v_mfma_f32_16x16x32_bf16 v[84:87], v[140:143], v[108:111], v[84:87]\n\t"
                "v_mfma_f32_16x16x32_bf16 v[88:91], v[144:147], v[96:99], v[88:91]\n\t"
                "v_mfma_f32_16x16x32_bf16 v[92:95], v[144:147], v[100:103], v[92:95]\n\t"
                "LHW%=:\n\t"
                "s_waitcnt lgkmcnt(0)\n\t"
                "s_barrier\n\t"
                "v_mfma_f32_16x16x32_bf16 v[24:27], v[148:151], v[112:115], v[24:27]\n\t"
                "v_add_u32 %[a01], %[sA], %[l16]\n\t"
                "v_xor_b32 %[a23], 0x1000, %[a01]\n\t"
                "ds_read_b128 v[96:99], %[a01] offset:0\n\t"
                "v_mfma_f32_16x16x32_bf16 v[40:43], v[148:151], v[116:119], v[40:43]\n\t"
                "v_add_u32 %[b0], %[sB], %[trb]\n\t"
                "v_xor_b32 %[b1], 32, %[b0]\n\t"
                "v_add_u32 %[b8], %[sS], %[trs]\n\t"
                "ds_read_b128 v[100:103], %[a01] offset:2048\n\t"
                "v_mfma_f32_16x16x32_bf16 v[56:59], v[148:151], v[120:123], v[56:59]\n\t"
                "ds_read_b128 v[104:107], %[a23] offset:0\n\t"
                "v_mfma_f32_16x16x32_bf16 v[72:75], v[148:151], v[124:127], v[72:75]\n\t"
                "ds_read_b128 v[108:111], %[a23] offset:2048\n\t"
                "v_mfma_f32_16x16x32_bf16 v[28:31], v[152:155], v[112:115], v[28:31]\n\t"
                "ds_read_b64_tr_b16 v[128:129], %[b0] offset:0\n\t"
                "ds_read_b64_tr_b16 v[130:131], %[b0] offset:1024\n\t"
                "v_mfma_f32_16x16x32_bf16 v[44:47], v[152:155], v[116:119], v[44:47]\n\t"
                "ds_read_b64_tr_b16 v[132:133], %[b1] offset:0\n\t"
                "ds_read_b64_tr_b16 v[134:135], %[b1] offset:1024\n\t"
                "v_mfma_f32_16x16x32_bf16 v[60:63], v[152:155], v[120:123], v[60:63]\n\t"
                "ds_read_b64_tr_b16 v[136:137], %[b0] offset:512\n\t"
                "ds_read_b64_tr_b16 v[138:139], %[b0] offset:1536\n\t"
                "v_mfma_f32_16x16x32_bf16 v[76:79], v[152:155], v[124:127], v[76:79]\n\t"
                "ds_read_b64_tr_b16 v[140:141], %[b1] offset:512\n\t"
                "ds_read_b64_tr_b16 v[142:143], %[b1] offset:1536\n\t"
                "v_mfma_f32_16x16x32_bf16 v[32:35], v[156:159], v[112:115], v[32:35]\n\t"
                "ds_read_b64_tr_b16 v[144:145], %[b8] offset:0\n\t"
                "ds_read_b64_tr_b16 v[146:147], %[b8] offset:256\n\t"
                "v_mfma_f32_16x16x32_bf16 v[48:51], v[156:159], v[116:119], v[48:51]\n\t"
                "v_mfma_f32_16x16x32_bf16 v[64:67], v[156:159], v[120:123], v[64:67]\n\t"
                "v_mfma_f32_16x16x32_bf16 v[80:83], v[156:159], v[124:127], v[80:83]\n\t"
                "v_mfma_f32_16x16x32_bf16 v[36:39], v[160:163], v[112:115], v[36:39]\n\t"
                "v_mfma_f32_16x16x32_bf16 v[52:55], v[160:163], v[116:119], v[52:55]\n\t"
                "v_mfma_f32_16x16x32_bf16 v[68:71], v[160:163], v[120:123], v[68:71]\n\t"
                "v_mfma_f32_16x16x32_bf16 v[84:87], v[160:163], v[124:127], v[84:87]\n\t"
                "v_mfma_f32_16x16x32_bf16 v[88:91], v[164:167], v[112:115], v[88:91]\n\t"
                "v_mfma_f32_16x16x32_bf16 v[92:95], v[164:167], v[116:119], v[92:95]\n\t"
                "s_waitcnt lgkmcnt(0)\n\t"
                "v_mfma_f32_16x16x32_bf16 v[24:27], v[128:131], v[96:99], v[24:27]\n\t"
                "ds_read_b128 v[112:115], %[a01] offset:1024\n\t"
                "v_mfma_f32_16x16x32_bf16 v[40:43], v[128:131], v[100:103], v[40:43]\n\t"
                "ds_read_b128 v[116:119], %[a01] offset:3072\n\t"
                "v_mfma_f32_16x16x32_bf16 v[56:59], v[128:131], v[104:107], v[56:59]\n\t"
                "ds_read_b128 v[120:123], %[a23] offset:1024\n\t"
                "v_mfma_f32_16x16x32_bf16 v[72:75], v[128:131], v[108:111], v[72:75]\n\t"
                "ds_read_b128 v[124:127], %[a23] offset:3072\n\t"
                "v_mfma_f32_16x16x32_bf16 v[28:31], v[132:135], v[96:99], v[28:31]\n\t"
                "ds_read_b64_tr_b16 v[148:149], %[b0] offset:9216\n\t"
                "ds_read_b64_tr_b16 v[150:151], %[b0] offset:10240\n\t"
                "v_mfma_f32_16x16x32_bf16 v[44:47], v[132:135], v[100:103], v[44:47]\n\t"
                "ds_read_b64_tr_b16 v[152:153], %[b1] offset:9216\n\t"
                "ds_read_b64_tr_b16 v[154:155], %[b1] offset:10240\n\t"
                "v_mfma_f32_16x16x32_bf16 v[60:63], v[132:135], v[104:107], v[60:63]\n\t"
                "ds_read_b64_tr_b16 v[156:157], %[b0] offset:9728\n\t"
                "ds_read_b64_tr_b16 v[158:159], %[b0] offset:10752\n\t"
                "v_mfma_f32_16x16x32_bf16 v[76:79], v[132:135], v[108:111], v[76:79]\n\t"
                "ds_read_b64_tr_b16 v[160:161], %[b1] offset:9728\n\t"
                "ds_read_b64_tr_b16 v[162:163], %[b1] offset:10752\n\t"
                "v_mfma_f32_16x16x32_bf16 v[32:35], v[136:139], v[96:99], v[32:35]\n\t"
                "ds_read_b64_tr_b16 v[164:165], %[b8] offset:9216\n\t"
                "ds_read_b64_tr_b16 v[166:167], %[b8] offset:9472\n\t"
                "v_mfma_f32_16x16x32_bf16 v[48:51], v[136:139], v[100:103], v[48:51]\n\t"
                "s_add_u32 %[sA], %[sA], 0x8000\n\t"
                "s_cmp_lt_u32 %[sA], 0x18000\n\t"
                "s_cselect_b32 %[sA], %[sA], %[wA]\n\t"
                "v_mfma_f32_16x16x32_bf16 v[64:67], v[136:139], v[104:107], v[64:67]\n\t"
                "s_add_u32 %[sS], %[sS], 18432\n\t"
                "s_cmp_lt_u32 %[sS], 161792\n\t"
                "s_cselect_b32 %[sS], %[sS], 106496\n\t"
                "v_mfma_f32_16x16x32_bf16 v[80:83], v[136:139], v[108:111], v[80:83]\n\t"
                "s_add_u32 %[sB], %[sS], %[sD]\n\t"
                "s_add_u32 %[sT], %[sT], 1\n\t"
                "s_cmp_eq_u32 %[sT], %[sE]\n\t"
                "v_mfma_f32_16x16x32_bf16 v[36:39], v[140:143], v[96:99], v[36:39]\n\t"
                "s_cbranch_scc1 LT%=\n\t"
                "s_cmp_lt_u32 %[sT], %[sK]\n\t"
                "v_mfma_f32_16x16x32_bf16 v[52:55], v[140:143], v[100:103], v[52:55]\n\t"
                "s_cbranch_scc0 LH%=\n\t"
                "s_branch LC%=\n\t"
                "LT%=:\n\t"
                "v_mfma_f32_16x16x32_bf16 v[52:55], v[140:143], v[100:103], v[52:55]\n\t"
                "v_add_u32 %[a01], %[sA], %[l16]\n\t"
                "v_xor_b32 %[a23], 0x1000, %[a01]\n\t"
                "v_mfma_f32_16x16x32_bf16 v[68:71], v[140:143], v[104:107], v[68:71]\n\t"
                "v_add_u32 %[b0], %[sB], %[trb]\n\t"
                "v_xor_b32 %[b1], 32, %[b0]\n\t"
                "v_add_u32 %[b8], %[sS], %[trs]\n\t"
                "v_mfma_f32_16x16x32_bf16 v[84:87], v[140:143], v[108:111], v[84:87]\n\t"
                "s_cmp_eq_u32 %[sO], 0\n\t"
                "s_cselect_b64 vcc, -1, 0\n\t"
                "v_mfma_f32_16x16x32_bf16 v[88:91], v[144:147], v[96:99], v[88:91]\n\t"
                "v_mfma_f32_16x16x32_bf16 v[92:95], v[144:147], v[100:103], v[92:95]\n\t"
                "s_waitcnt lgkmcnt(0)\n\t"
                "s_barrier\n\t"
                "v_mfma_f32_16x16x32_bf16 v[24:27], v[148:151], v[112:115], v[24:27]\n\t"
                "ds_read_b128 v[96:99], %[a01] offset:0\n\t"
                "ds_read_b128 v[100:103], %[a01] offset:2048\n\t"
                "v_mfma_f32_16x16x32_bf16 v[40:43], v[148:151], v[116:119], v[40:43]\n\t"
                "ds_read_b64_tr_b16 v[144:145], %[b0] offset:0\n\t"
                "ds_read_b64_tr_b16 v[146:147], %[b0] offset:1024\n\t"
                "v_mfma_f32_16x16x32_bf16 v[56:59], v[148:151], v[120:123], v[56:59]\n\t"
                "ds_read_b128 v[128:131], %[a01] offset:1024\n\t"
                "ds_read_b128 v[132:135], %[a01] offset:3072\n\t"
                "v_mfma_f32_16x16x32_bf16 v[72:75], v[148:151], v[124:127], v[72:75]\n\t"
                "ds_read_b64_tr_b16 v[148:149], %[b0] offset:9216\n\t"
                "ds_read_b64_tr_b16 v[150:151], %[b0] offset:10240\n\t"
                "v_mfma_f32_16x16x32_bf16 v[28:31], v[152:155], v[112:115], v[28:31]\n\t"
                "ds_read_b128 v[104:107], %[a23] offset:0\n\t"
                "ds_read_b128 v[108:111], %[a23] offset:2048\n\t"
                "v_mfma_f32_16x16x32_bf16 v[44:47], v[152:155], v[116:119], v[44:47]\n\t"
                "ds_read_b128 v[136:139], %[a23] offset:1024\n\t"
                "ds_read_b128 v[140:143], %[a23] offset:3072\n\t"
                "v_mfma_f32_16x16x32_bf16 v[60:63], v[152:155], v[120:123], v[60:63]\n\t"
                "v_mfma_f32_16x16x32_bf16 v[76:79], v[152:155], v[124:127], v[76:79]\n\t"
                "ds_read_b64_tr_b16 v[152:153], %[b1] offset:0\n\t"
                "ds_read_b64_tr_b16 v[154:155], %[b1] offset:1024\n\t"
                "s_waitcnt lgkmcnt(10)\n\t"
                "v_mfma_f32_16x16x32_bf16 v[24:27], v[144:147], v[96:99], v[24:27]\n\t"
                "v_mfma_f32_16x16x32_bf16 v[40:43], v[144:147], v[100:103], v[40:43]\n\t"
                "s_waitcnt lgkmcnt(6)\n\t"
                "v_mfma_f32_16x16x32_bf16 v[24:27], v[148:151], v[128:131], v[24:27]\n\t"
                "v_mfma_f32_16x16x32_bf16 v[40:43], v[148:151], v[132:135], v[40:43]\n\t"
                "s_waitcnt lgkmcnt(5)\n\t"
                "v_mfma_f32_16x16x32_bf16 v[56:59], v[144:147], v[104:107], v[56:59]\n\t"
                "s_waitcnt lgkmcnt(4)\n\t"
                "v_mfma_f32_16x16x32_bf16 v[72:75], v[144:147], v[108:111], v[72:75]\n\t"
                "ds_read_b64_tr_b16 v[144:145], %[b1] offset:9216\n\t"
                "ds_read_b64_tr_b16 v[146:147], %[b1] offset:10240\n\t"
                "s_waitcnt lgkmcnt(5)\n\t"
                "v_mfma_f32_16x16x32_bf16 v[56:59], v[148:151], v[136:139], v[56:59]\n\t"
                "v_cvt_pk_bf16_f32 v24, v24, v25\n\t"
                "v_cvt_pk_bf16_f32 v25, v26, v27\n\t"
                "s_waitcnt lgkmcnt(4)\n\t"
                "v_mfma_f32_16x16x32_bf16 v[72:75], v[148:151], v[140:143], v[72:75]\n\t"
                "ds_read_b64_tr_b16 v[148:149], %[b0] offset:512\n\t"
                "ds_read_b64_tr_b16 v[150:151], %[b0] offset:1536\n\t"
                "v_cvt_pk_bf16_f32 v26, v40, v41\n\t"
                "v_cvt_pk_bf16_f32 v27, v42, v43\n\t"
                "s_waitcnt lgkmcnt(4)\n\t"
                "v_mfma_f32_16x16x32_bf16 v[28:31], v[152:155], v[96:99], v[28:31]\n\t"
                "s_mov_b32 %[st], 0\n\t"
                "buffer_store_dwordx4 v[24:27], %[pd], %[rs], %[st] offen sc1\n\t"
                "s_mov_b64 exec, vcc\n\t"
                "ds_write_b128 %[l16], v[24:27] offset:0\n\t"
                "s_mov_b64 exec, -1\n\t"
                "v_cvt_pk_bf16_f32 v56, v56, v57\n\t"
                "v_cvt_pk_bf16_f32 v57, v58, v59\n\t"
                "v_mfma_f32_16x16x32_bf16 v[44:47], v[152:155], v[100:103], v[44:47]\n\t"
                "v_cvt_pk_bf16_f32 v58, v72, v73\n\t"
                "v_cvt_pk_bf16_f32 v59, v74, v75\n\t"
                "s_waitcnt lgkmcnt(2)\n\t"
                "v_mfma_f32_16x16x32_bf16 v[28:31], v[144:147], v[128:131], v[28:31]\n\t"
                "s_mov_b32 %[st], 32768\n\t"
                "buffer_store_dwordx4 v[56:59], %[pd], %[rs], %[st] offen sc1\n\t"
                "s_mov_b64 exec, vcc\n\t"
                "ds_write_b128 %[l16], v[56:59] offset:4096\n\t"
                "s_mov_b64 exec, -1\n\t"
                "v_mfma_f32_16x16x32_bf16 v[44:47], v[144:147], v[132:135], v[44:47]\n\t"
                "v_mfma_f32_16x16x32_bf16 v[60:63], v[152:155], v[104:107], v[60:63]\n\t"
                "v_mfma_f32_16x16x32_bf16 v[76:79], v[152:155], v[108:111], v[76:79]\n\t"
                "ds_read_b64_tr_b16 v[152:153], %[b0] offset:9728\n\t"
                "ds_read_b64_tr_b16 v[154:155], %[b0] offset:10752\n\t"
                "v_mfma_f32_16x16x32_bf16 v[60:63], v[144:147], v[136:139], v[60:63]\n\t"
                "v_mfma_f32_16x16x32_bf16 v[76:79], v[144:147], v[140:143], v[76:79]\n\t"
                "ds_read_b64_tr_b16 v[144:145], %[b1] offset:512\n\t"
                "ds_read_b64_tr_b16 v[146:147], %[b1] offset:1536\n\t"
                "v_cvt_pk_bf16_f32 v28, v28, v29\n\t"
                "v_cvt_pk_bf16_f32 v29, v30, v31\n\t"
                "v_mfma_f32_16x16x32_bf16 v[32:35], v[156:159], v[112:115], v[32:35]\n\t"
                "v_cvt_pk_bf16_f32 v30, v44, v45\n\t"
                "v_cvt_pk_bf16_f32 v31, v46, v47\n\t"
                "v_mfma_f32_16x16x32_bf16 v[48:51], v[156:159], v[116:119], v[48:51]\n\t"
                "s_mov_b32 %[st], 8192\n\t"
                "buffer_store_dwordx4 v[28:31], %[pd], %[rs], %[st] offen sc1\n\t"
                "s_mov_b64 exec, vcc\n\t"
                "ds_write_b128 %[l16], v[28:31] offset:1024\n\t"
                "s_mov_b64 exec, -1\n\t"
                "v_cvt_pk_bf16_f32 v60, v60, v61\n\t"
                "v_cvt_pk_bf16_f32 v61, v62, v63\n\t"
                "v_mfma_f32_16x16x32_bf16 v[64:67], v[156:159], v[120:123], v[64:67]\n\t"
                "v_cvt_pk_bf16_f32 v62, v76, v77\n\t"
                "v_cvt_pk_bf16_f32 v63, v78, v79\n\t"
                "v_mfma_f32_16x16x32_bf16 v[80:83], v[156:159], v[124:127], v[80:83]\n\t"
                "ds_read_b64_tr_b16 v[156:157], %[b1] offset:9728\n\t"
                "ds_read_b64_tr_b16 v[158:159], %[b1] offset:10752\n\t"
                "s_mov_b32 %[st], 40960\n\t"
                "buffer_store_dwordx4 v[60:63], %[pd], %[rs], %[st] offen sc1\n\t"
                "s_mov_b64 exec, vcc\n\t"
                "ds_write_b128 %[l16], v[60:63] offset:5120\n\t"
                "s_mov_b64 exec, -1\n\t"
                "s_waitcnt lgkmcnt(6)\n\t"
                "v_mfma_f32_16x16x32_bf16 v[32:35], v[148:151], v[96:99], v[32:35]\n\t"
                "v_mfma_f32_16x16x32_bf16 v[48:51], v[148:151], v[100:103], v[48:51]\n\t"
                "s_waitcnt lgkmcnt(4)\n\t"
                "v_mfma_f32_16x16x32_bf16 v[32:35], v[152:155], v[128:131], v[32:35]\n\t"
                "v_mfma_f32_16x16x32_bf16 v[48:51], v[152:155], v[132:135], v[48:51]\n\t"
                "v_mfma_f32_16x16x32_bf16 v[64:67], v[148:151], v[104:107], v[64:67]\n\t"
                "v_mfma_f32_16x16x32_bf16 v[80:83], v[148:151], v[108:111], v[80:83]\n\t"
                "ds_read_b64_tr_b16 v[148:149], %[b8] offset:0\n\t"
                "ds_read_b64_tr_b16 v[150:151], %[b8] offset:256\n\t"
                "v_mfma_f32_16x16x32_bf16 v[64:67], v[152:155], v[136:139], v[64:67]\n\t"
                "v_mfma_f32_16x16x32_bf16 v[80:83], v[152:155], v[140:143], v[80:83]\n\t"
                "ds_read_b64_tr_b16 v[152:153], %[b8] offset:9216\n\t"
                "ds_read_b64_tr_b16 v[154:155], %[b8] offset:9472\n\t"
                "v_cvt_pk_bf16_f32 v32, v32, v33\n\t"
                "v_cvt_pk_bf16_f32 v33, v34, v35\n\t"
                "v_mfma_f32_16x16x32_bf16 v[36:39], v[160:163], v[112:115], v[36:39]\n\t"
                "v_cvt_pk_bf16_f32 v34, v48, v49\n\t"
                "v_cvt_pk_bf16_f32 v35, v50, v51\n\t"
                "v_mfma_f32_16x16x32_bf16 v[52:55], v[160:163], v[116:119], v[52:55]\n\t"
                "s_mov_b32 %[st], 16384\n\t"
                "buffer_store_dwordx4 v[32:35], %[pd], %[rs], %[st] offen sc1\n\t"
                "s_mov_b64 exec, vcc\n\t"
                "ds_write_b128 %[l16], v[32:35] offset:2048\n\t"
                "s_mov_b64 exec, -1\n\t"
                "v_cvt_pk_bf16_f32 v64, v64, v65\n\t"
                "v_cvt_pk_bf16_f32 v65, v66, v67\n\t"
                "v_mfma_f32_16x16x32_bf16 v[68:71], v[160:163], v[120:123], v[68:71]\n\t"
                "v_cvt_pk_bf16_f32 v66, v80, v81\n\t"
                "v_cvt_pk_bf16_f32 v67, v82, v83\n\t"
                "v_mfma_f32_16x16x32_bf16 v[84:87], v[160:163], v[124:127], v[84:87]\n\t"
                "s_mov_b32 %[st], 49152\n\t"
                "buffer_store_dwordx4 v[64:67], %[pd], %[rs], %[st] offen sc1\n\t"
                "s_mov_b64 exec, vcc\n\t"
                "ds_write_b128 %[l16], v[64:67] offset:6144\n\t"
                "s_mov_b64 exec, -1\n\t"
                "s_waitcnt lgkmcnt(6)\n\t"
                "v_mfma_f32_16x16x32_bf16 v[36:39], v[144:147], v[96:99], v[36:39]\n\t"
                "v_mfma_f32_16x16x32_bf16 v[52:55], v[144:147], v[100:103], v[52:55]\n\t"
                "s_waitcnt lgkmcnt(4)\n\t"
                "v_mfma_f32_16x16x32_bf16 v[36:39], v[156:159], v[128:131], v[36:39]\n\t"
                "v_mfma_f32_16x16x32_bf16 v[52:55], v[156:159], v[132:135], v[52:55]\n\t"
                "v_mfma_f32_16x16x32_bf16 v[68:71], v[144:147], v[104:107], v[68:71]\n\t"
                "v_mfma_f32_16x16x32_bf16 v[84:87], v[144:147], v[108:111], v[84:87]\n\t"
                "v_mfma_f32_16x16x32_bf16 v[68:71], v[156:159], v[136:139], v[68:71]\n\t"
                "v_mfma_f32_16x16x32_bf16 v[84:87], v[156:159], v[140:143], v[84:87]\n\t"
                "v_mfma_f32_16x16x32_bf16 v[88:91], v[164:167], v[112:115], v[88:91]\n\t"
                "v_mfma_f32_16x16x32_bf16 v[92:95], v[164:167], v[116:119], v[92:95]\n\t"
                "s_waitcnt lgkmcnt(2)\n\t"
                "v_mfma_f32_16x16x32_bf16 v[88:91], v[148:151], v[96:99], v[88:91]\n\t"
                "v_cvt_pk_bf16_f32 v36, v36, v37\n\t"
                "v_cvt_pk_bf16_f32 v37, v38, v39\n\t"
                "v_mfma_f32_16x16x32_bf16 v[92:95], v[148:151], v[100:103], v[92:95]\n\t"
                "v_cvt_pk_bf16_f32 v38, v52, v53\n\t"
                "v_cvt_pk_bf16_f32 v39, v54, v55\n\t"
                "s_waitcnt lgkmcnt(0)\n\t"
                "v_mfma_f32_16x16x32_bf16 v[88:91], v[152:155], v[128:131], v[88:91]\n\t"
                "s_mov_b32 %[st], 24576\n\t"
                "buffer_store_dwordx4 v[36:39], %[pd], %[rs], %[st] offen sc1\n\t"
                "s_mov_b64 exec, vcc\n\t"
                "ds_write_b128 %[l16], v[36:39] offset:3072\n\t"
                "s_mov_b64 exec, -1\n\t"
                "v_cvt_pk_bf16_f32 v68, v68, v69\n\t"
                "v_cvt_pk_bf16_f32 v69, v70, v71\n\t"
                "v_mfma_f32_16x16x32_bf16 v[92:95], v[152:155], v[132:135], v[92:95]\n\t"
                "v_cvt_pk_bf16_f32 v70, v84, v85\n\t"
                "v_cvt_pk_bf16_f32 v71, v86, v87\n\t"
                "s_mov_b32 %[st], 57344\n\t"
                "buffer_store_dwordx4 v[68:71], %[pd], %[rs], %[st] offen sc1\n\t"
                "s_mov_b64 exec, vcc\n\t"
                "ds_write_b128 %[l16], v[68:71] offset:7168\n\t"
                "s_mov_b64 exec, -1\n\t"
                "s_nop 0\n\t"
                "v_cvt_pk_bf16_f32 v88, v88, v89\n\t"
                "v_cvt_pk_bf16_f32 v89, v90, v91\n\t"
                "v_cvt_pk_bf16_f32 v90, v92, v93\n\t"
                "v_cvt_pk_bf16_f32 v91, v94, v95\n\t"
                "s_mov_b32 %[st], 65536\n\t"
                "buffer_store_dwordx4 v[88:91], %[pd], %[rs], %[st] offen sc1\n\t"
                "s_mov_b64 exec, vcc\n\t"
                "ds_write_b128 %[l16], v[88:91] offset:8192\n\t"
                "s_mov_b64 exec, -1\n\t"
                : [a01] "=&v"(a01), [a23] "=&v"(a23), [b0] "=&v"(b0), [b1] "=&v"(b1), [b8] "=&v"(b8),
                  [sA] "+&s"(sA), [sB] "+&s"(sB), [sS] "+&s"(sS), [sT] "+&s"(sT), [st] "=&s"(st)
                : [l16] "v"(l16), [rowl] "v"(rowl), [trb] "v"(trb), [trs] "v"(trs), [pd] "v"(pd), [wA] "s"(wA), [sD] "s"(sD), [sE] "s"(sE), [sK] "s"(sK), [sO] "s"(sO), [rs] "s"(rs_ws)
                : "memory", "scc", "vcc", "v24", "v25", "v26", "v27", "v28", "v29", "v30", "v31", "v32", "v33", "v34", "v35", "v36", "v37", "v38", "v39", "v40", "v41", "v42", "v43", "v44", "v45", "v46", "v47", "v48", "v49", "v50", "v51", "v52", "v53", "v54", "v55", "v56", "v57", "v58", "v59", "v60", "v61", "v62", "v63", "v64", "v65", "v66", "v67", "v68", "v69", "v70", "v71", "v72", "v73", "v74", "v75", "v76", "v77", "v78", "v79", "v80", "v81", "v82", "v83", "v84", "v85", "v86", "v87", "v88", "v89", "v90", "v91", "v92", "v93", "v94", "v95", "v96", "v97", "v98", "v99", "v100", "v101", "v102", "v103", "v104", "v105", "v106", "v107", "v108", "v109", "v110", "v111", "v112", "v113", "v114", "v115", "v116", "v117", "v118", "v119", "v120", "v121", "v122", "v123", "v124", "v125", "v126", "v127", "v128", "v129", "v130", "v131", "v132", "v133", "v134", "v135", "v136", "v137", "v138", "v139", "v140", "v141", "v142", "v143", "v144", "v145", "v146", "v147", "v148", "v149", "v150", "v151", "v152", "v153", "v154", "v155", "v156", "v157", "v158", "v159", "v160", "v161", "v162", "v163", "v164", "v165", "v166", "v167");
            STAMP(4);
            tag = lds_ld<u32>(L_TAG);
            WAIT_L(0); BAR();
            if (w != j) {
                WAIT_V(0);
                if (l < PW_) st_flag(ws + WS_FP + ((g * 8 + w) * 8 + j) * PS + 4 * l, tag);
            }
            STAMP(5);
        }
    } else if (w < 10) {
        const int ld = w - 8;
        __builtin_amdgcn_s_setprio(LDPRIO);
        const rsrc_t rs_x = mk_rsrc(x, S_ * D_ * 2);
        const u32 sw0 = 16u * ((l & 7) ^ q), sw1 = sw0 ^ 64u;
        const u32 a8 = (l >> 2) & 7, sA = (u32)(row0 + 128 * ld) * 1152u;
        const u32 lA = ld * 16384u, lB = L_B + ld * 9216u;
        const rsrc_t rs_b = mk_rsrc((const char*)(is_ab ? ((j >> 2) ? Di : Dr) : w1) - 4096, (is_ab ? D_ * D_ * 2 : D_ * F_ * 2) + 4096);
        const u32 vA0 = (is_ab ? (u32)(8 * (l >> 5) + ((l >> 3) & 3)) : (u32)(l >> 3)) * 1152u + sw0, vA1 = vA0 ^ 64u;
        const u32 sB = (u32)(16 * ld) * 9216u + (u32)(576 * j + 192 * nb) * 2u + 4096u, lB1 = L_B + ld * 6144u;
        u32 vB, vD[9];
        auto issueB = [&](int u, u32 slot) {
            u32 so = sB + (u32)u * (32u * 9216u), lb = lB1 + slot;
            asm volatile("" : "+s"(lb), "+s"(so));
            sfor<6>([&](auto i) { dma<NT1, i>(rs_b, lb, vB, so + (u32)(i / 3) * (4u * 9216u) + (u32)(i % 3) * 128u); });
        };
        auto issueA = [&](int s, auto i0, auto i1) {
            u32 lb = (u32)(s % 3) * 32768u + lA, so = sA + (u32)s * 128u;
            asm volatile("" : "+s"(lb), "+s"(so));
            sfor<16>([&](auto i) { if constexpr (i >= i0 && i < i1) dma<NTX, i>(rs_x, lb, (i & 1) ? vA1 : vA0, so + i * (8u * 1152u)); });
        };
        auto issueG = [&](int s, auto i0, auto i1) {
            const u32 sl = (u32)(s % 3);
            u32 lb = sl * 32768u + lA, so = sA + (u32)s * 128u, lbb = lB + sl * 18432u, sd = (u32)s * 128u;
            asm volatile("" : "+s"(lb), "+s"(so), "+s"(lbb), "+s"(sd));
            sfor<16>([&](auto i) { if constexpr (i >= i0 && i < i1) dma<NTX, i>(rs_x, lb, (i & 1) ? vA1 : vA0, so + (u32)(32 * (i >> 2) + 16 * (i & 1) + 4 * ((i >> 1) & 1)) * 1152u); });
            sfor<9>([&](auto i) { if constexpr (i + 16 >= i0 && i + 16 < i1) dma<0, i>(rs_b, lbb, vD[i], sd + (i & 3) * 1024u); });
        };
        TR3();
        if (!is_ab) {
            TR3();
            vB = (8u * (a8 >> 2) + (a8 & 3)) * 9216u + (u32)(l >> 5) * 64u + 32u * (((l >> 1) & 1) ^ (a8 >> 2)) + 16u * (l & 1);
            issueB(0, 0); issueB(1, 12288);
            issueB(2, 24576); issueA(1, IC<0>, IC<(PRE < 22 ? PRE - 6 : 16)>);
            if (PRE == 28) issueB(3, 36864);
        } else {
            TR3();
            const u32 vB0 = (u32)(l >> 3) * 1152u + (ld ? sw1 : sw0);
#pragma unroll
            for (int i = 0; i < 9; ++i) {
                const int p = 9 * ld + i;
                vD[i] = (vB0 ^ (64u * (i & 1))) + (144u * cn + colmap(cn, p >> 1) + 8u * (p & 1)) * 1152u + 4096u - (i & 3) * 1024u;
            }
            issueG(0, IC<16>, IC<25>); issueG(1, IC<0>, IC<PREG>);
        }
        TR3();
        const rsrc_t rs_c = mk_rsrc((j >> 2) ? Ci : Cr, S_ * S_ * 2);
        const rsrc_t rs_w2 = mk_rsrc(w2, F_ * D_ * 2);
        const u32 sC = (u32)(256 * (mb & XM) + 128 * ld) * 4096u + 1024u * (j & 3);
        const int rot = 4 * (mb >> 2);
        const u32 sH = WS_H + (u32)((j * 8 + mb) * 9) * 32768u + ld * 16384u, sW = (u32)(576 * j + 32 * ld) * 1152u + 288u * cn;
        const u32 sAB = WS_AB + (u32)((j * 4 + cn) * 16 + ld) * 9216u;
        u32 vC0, vC1, vW0, vW1, vWs, fo;
        auto addr2 = [&]() {
            u32 lo = l;
            asm volatile("" : "+v"(lo) :: "memory");
            const bool hi = lo >= 32u;
            vC0 = (lo >> 3) * 4096u + sw0; vC1 = vC0 ^ 64u;
            const u32 vW = a8 * 1152u + 32u * (((lo >> 1) & 1) ^ (a8 >> 2)) + 16u * (lo & 1);
            vW0 = vW + 2u * (hi ? colmap(cn, 2) : colmap(cn, 0)); vW1 = vW + 2u * (hi ? colmap(cn, 6) : colmap(cn, 4));
            vWs = (16u * ((lo >> 4) & 1) + 8u * (lo >> 5) + ((lo >> 1) & 7)) * 1152u + 2u * colmap(cn, 8) + 16u * (lo & 1);
            const u32 f0 = WS_FAB + (u32)((j * 8 + cn) * 4) * FS + (16u * (lo >> 3) + ((lo >> 1) & 3)) * FS, f1 = WS_FH + (u32)((j * 8 + mb) * 16) * FS + (((lo - 16u) >> 1) & 15) * FS;
            fo = (lo < 16u ? f0 : f1) + HS * (lo & 1);
            asm volatile("" : "+v"(vC0), "+v"(vC1), "+v"(vW0), "+v"(vW1), "+v"(vWs), "+v"(fo));
        };
        addr2();
        auto issueC = [&](u32 lb, u32 so) { sfor<16>([&](auto i) { dma<NTC, i>(rs_c, lb, (i & 1) ? vC1 : vC0, so + i * XC); }); };
        auto issueH = [&](u32 lb, u32 so) { sfor<16>([&](auto i) { dma<XH, i>(rs_ws, lb, l16, so + i * XH2); }); };
        auto issueW = [&](u32 lb, u32 so) {
            sfor<8>([&](auto i) { dma<NT2, i>(rs_w2, lb, (i >> 2) ? vW1 : vW0, so + (u32)(16 * (i & 1) + 8 * ((i >> 1) & 1)) * 1152u); });
            dma<NT2, 8>(rs_w2, lb, vWs, so);
        };
        auto issueAB = [&](u32 lb, u32 so) { sfor<9>([&](auto i) { dma<XB, i>(rs_ws, lb, l16, so + i * XB2); }); };
        TR3();
        if (!is_ab) asm volatile("s_waitcnt vmcnt(%0) ; h" :: "i"(PRE) : "memory"); else asm volatile("s_waitcnt vmcnt(%0) ; ab" :: "i"(PREG) : "memory");
        TR3(); ABAR();
        TR3();
        STAMP(1);
        u32 fv;
        unsigned long long seen = (is_ab ? 255ull << (2 * rot) : 63ull << (16 + 6 * nb)) | ~0ull << 34;
        auto ask = [&](u32 bit) {
            asm volatile("s_andn2_b64 exec, %3, %4\n\tglobal_load_dword %0, %1, %2 sc1\n\ts_mov_b64 exec, -1"
                         : "+v"(fv) : "v"(fo), "s"(ws), "s"(ASKM), "s"(seen) : "memory", "scc");
        };
        if (!is_ab) {
            issueA(1, IC<(PRE < 22 ? PRE - 6 : 16)>, IC<16>);
            if (PRE < 28) issueB(3, 36864);
            issueB(4, 49152); issueA(2, IC<0>, IC<8>);
            TR3(); WAIT_V(14); TR3(); ABAR();
            TR3();
            issueA(2, IC<8>, IC<16>);
            const u32 sV = (u32)(256 * (mb & XM) + 8 * ((16 * (2 * nb + ld)) / 3)) * 4096u + 1024u * (j & 3) + 128u * (4 - rot);
            auto warmL = [&](int n0, int n1) {
#pragma unroll
                for (int n = 0; n < 30; ++n) if (n >= n0 && n < n1)
                    asm volatile("buffer_load_dwordx4 v[152:155], %0, %1, %2 offen offset:%3" :: "v"(vC0), "s"(rs_c), "s"((n < 6 ? sV ^ 512u : sV) + (u32)(n % 6) * 32768u), "i"(n < 6 ? 384 : ((n / 6 + 2) & 3) * 128)
                                 : "memory", "v152", "v153", "v154", "v155");
            };
            u32 bs = 0;
#pragma unroll 1
            for (int s = 0; s <= 6; ++s) {
                if (s > 0) {
                    if (s == EW + 1 || s == EW + 2) asm volatile("s_waitcnt vmcnt(%0)" :: "i"(22 + WN(EWN)) : "memory"); else WAIT_V(22);
                    ABAR();
                }
                issueB(2 * s + 5, bs);
                bs = bs == 49152u ? 0u : bs + 12288u;
                if (s < 6) { issueB(2 * s + 6, bs); issueA(s + 3, IC<0>, IC<16>); }
                bs = bs == 49152u ? 0u : bs + 12288u;
                if (s == EW) warmL(0, EWN);
                if (s == EW + 1) warmL(EWN, 2 * EWN);
            }
            warmL(LW0, LW1);
            asm volatile("s_waitcnt vmcnt(%0)" :: "i"(WN(LW1 - LW0)) : "memory");
            ABAR();
            tag = __builtin_amdgcn_readfirstlane(lds_ld<u32>(L_TAG)); fv = ~tag;
            issueW(lB + 36864u, sW + (u32)(3 * nb) * 73728u);
            warmL(LW1, LW2);
            ABAR();
            warmL(LW2, LW3);
            asm volatile("s_waitcnt vmcnt(%0)" :: "i"(WN(LW3 - LW1)) : "memory");
            __builtin_amdgcn_s_sleep(WL8);
            issueW(lB, sW + (u32)(3 * nb + 1) * 73728u);
            ABAR();
            STAMP(2);
            issueW(lB + 18432u, sW + (u32)(3 * nb + 2) * 73728u);
            WAIT_V(9);
            ABAR();
        } else {
            issueG(1, IC<PREG>, IC<25>); issueG(2, IC<0>, IC<12>);
            TR3(); WAIT_V(12); TR3(); ABAR();
            TR3();
            issueG(2, IC<12>, IC<25>);
#pragma unroll 1
            for (int s = 0; s <= 5; ++s) {
                if (s > 0) { WAIT_V(25); ABAR(); }
                issueG(s + 3, IC<0>, IC<25>);
            }
            WAIT_V(25); ABAR();
            issueC(lA, sC + 128u * rot);
            WAIT_V(16); ABAR();
            tag = __builtin_amdgcn_readfirstlane(lds_ld<u32>(L_TAG)); fv = ~tag;
            issueC(lA + 32768u, sC + 128u * (rot + 1));
            ABAR();
            issueC(lA + 65536u, sC + 128u * (rot + 2));
            WAIT_V(32); ABAR();
            STAMP(2);
            WAIT_V(16); ABAR();
        }
        auto request = [&](u32 bit) { if (((u32)(seen >> bit) & 3u) != 3u) ask(bit); };
        auto await = [&](u32 bit) {
            if (((u32)(seen >> bit) & 3u) != 3u) {
#pragma unroll 1
                for (int it = 0; it < SPIN_MAX; ++it) {
                    ask(bit);
                    asm volatile("s_waitcnt vmcnt(0)" : "+v"(fv) :: "memory");
                    seen |= __builtin_amdgcn_ballot_w64(fv == tag);
                    if (((u32)(seen >> bit) & 3u) == 3u) break;
                    __builtin_amdgcn_s_sleep(AS);
                }
            }
        };
        u32 ct = (is_ab ? 0x65473u : 0x62514073u) ^ (rot ? 0x44444444u : 0u);
        unsigned long long kt = is_ab ? 0xf852741630ull : nb == 0 ? 0xf857463ull : nb == 1 ? 0xf827160ull : 0xf524130ull;
        u32 kc = ct & 7u, ks = (u32)kt & 15u;
        TR2(); TR2();
        ask(0);
        issueC(lA, sC + 128u * kc);
        TR2();
        asm volatile("s_waitcnt vmcnt(16) ; warm-up retired" : "+v"(fv) :: "memory");
        TR2();
        seen |= __builtin_amdgcn_ballot_w64(fv == tag);
        ABAR();
        STAMP(3);
        {
            await(2 * kc);
            STAMP(4);
            issueAB(lB + 36864u, sAB + kc * 18432u);
        }
        u32 la = lA + 32768u, lb = lB, o1, o2;
        auto adv = [&]() { la = (la == lA + 65536u) ? lA : la + 32768u; lb = (lb == lB + 36864u) ? lB : lb + 18432u; };
        ct >>= 4; kc = ct & 7u; o1 = sC + 128u * kc; o2 = sAB + kc * 18432u;
#pragma unroll 1
        for (int t = 4 - c0; t < 8; ++t) {
            TR2();
            await(2 * kc);
            TR2();
            ct >>= 4; kc = ct & 7u;
            request(t == 7 ? 16 + 2 * ks : 2 * kc);
            issueC(la, o1); issueAB(lb, o2);
            TR2();
            adv(); o1 = sC + 128u * kc; o2 = sAB + kc * 18432u;
            asm volatile("s_waitcnt vmcnt(25)" : "+s"(la), "+s"(lb), "+s"(o1), "+s"(o2), "+v"(fv) :: "memory");
            TR2();
            seen |= __builtin_amdgcn_ballot_w64(fv == tag);
            asm volatile("" : "+s"(seen));
            ABAR();
        }
        o1 = sH + ks * 32768u; o2 = sW + ks * 73728u;
        STAMP(5);
#pragma unroll 1
        do {
            TR2();
            await(16 + 2 * ks);
            TR2();
            kt >>= 4; ks = (u32)kt & 15u;
            request(16 + 2 * ks);
            issueH(la, o1); issueW(lb, o2);
            TR2();
            adv(); o1 = sH + ks * 32768u; o2 = sW + ks * 73728u;
            asm volatile("s_waitcnt vmcnt(25)" : "+s"(la), "+s"(lb), "+s"(o1), "+s"(o2), "+v"(fv) :: "memory");
            TR2();
            seen |= __builtin_amdgcn_ballot_w64(fv == tag);
            asm volatile("" : "+s"(seen));
            ABAR();
        } while (ks != 15u);
        asm volatile("s_waitcnt vmcnt(0)" : "+v"(fv) :: "memory");
        ABAR();
        ABAR();
    } else {
        const int ax = w - 10;
        __builtin_amdgcn_s_setprio(AXPRIO);
        u32x4 sg[2];
        const u32 sgo = WS_STAT + (u32)(row0 + 128 * ax + l) * 16u;
        auto sg_load = [&]() { sg[0] = __builtin_amdgcn_raw_buffer_load_b128(rs_ws, sgo, 0, 16); sg[1] = __builtin_amdgcn_raw_buffer_load_b128(rs_ws, sgo + 1024, 0, 16); };
        auto sg_done = [&]() {
#pragma unroll 1
            for (int it = 0; it < SPIN_MAX; ++it) {
                if (__all(sg[0][1] == tag && sg[0][3] == tag && sg[1][1] == tag && sg[1][3] == tag)) break;
                __builtin_amdgcn_s_sleep(1);
                sg_load();
            }
            const u32 sgn = (is_ab && (j >> 2)) ? 0x80000000u : 0u;
            const u32 so = L_STAT + 512u * ax + (is_ab ? 4u * l : 16u * r16 + 4u * q);
            lds_st<u32>(so, sg[0][0]); lds_st<u32>(so + 1024, sg[0][2] ^ sgn);
            lds_st<u32>(so + 256, sg[1][0]); lds_st<u32>(so + 1280, sg[1][2] ^ sgn);
        };
        auto warm = [&](const char* sp, u32 vo) {
            asm volatile("global_load_dwordx4 v[152:155], %0, %1" WNT :: "v"(vo), "s"(sp) : "memory", "v152", "v153", "v154", "v155");
        };
        auto warm1 = [&](const char* sp, u32 vo) { asm volatile("global_load_dword v152, %0, %1" :: "v"(vo), "s"(sp) : "memory", "v152", "v153", "v154", "v155"); };
        const char* pc = (const char*)((j >> 2) ? Ci : Cr) + (size_t)(256 * mb + 2 * nb) * 4096u + 1024u * (j & 3);
        const u32 vc0 = (u32)(l >> 5) * 4096u + 16u * (l & 31) + 512u * (mb >> 2), vc1 = vc0 ^ 512u;
        auto warmC = [&](int n0, int n1) {
#pragma unroll
            for (int n = 0; n < 86; ++n) if (n >= n0 && n < n1) warm(pc + ((n % 43 == 42 && nb == 2) ? 41 : n % 43) * 24576, n < 43 ? vc0 : vc1);
        };
        u32x4 wv[27];
        auto wload = [&]() {
            const rsrc_t rs_w1 = mk_rsrc(w1, D_ * F_ * 2);
            const u32 vo = (u32)(l >> 3) * 9216u + 16u * (l & 7), so = (u32)(72 * mb) * 9216u + (u32)(576 * j + 192 * nb) * 2u;
#pragma unroll
            for (int i = 0; i < 27; ++i) wv[i] = __builtin_amdgcn_raw_buffer_load_b128(rs_w1, vo + (u32)(i % 3) * 128u, so + (u32)(i / 3) * (8u * 9216u), NT1);
        };
        const u32 epoch = __builtin_amdgcn_raw_buffer_load_b32(rs_ws, WS_EPOCH, 0, 16);
        u32 xr[18];
        {
            const rsrc_t rs_x = mk_rsrc(x, S_ * D_ * 2);
            const u32 ro = (u32)(bid * 8 + 4 * ax) * 1152u;
#pragma unroll
            for (int i = 0; i < 4; ++i) {
                const u32x4 t = __builtin_amdgcn_raw_buffer_load_b128(rs_x, (u32)q * 1152u + 16u * r16 + 256u * i, ro, 0);
                xr[4 * i] = t[0]; xr[4 * i + 1] = t[1]; xr[4 * i + 2] = t[2]; xr[4 * i + 3] = t[3];
            }
            const u32x2 t = __builtin_amdgcn_raw_buffer_load_b64(rs_x, (u32)q * 1152u + 8u * r16 + 1024u, ro, 0);
            xr[16] = t[0]; xr[17] = t[1];
        }
        auto stats = [&]() {
            SB();
            float s0 = 0.f, s1 = 0.f;
#pragma unroll
            for (int i = 0; i < 9; ++i) { s0 = dot1(xr[2 * i], s0); s1 = dot1(xr[2 * i + 1], s1); }
            const float nm = row16_sum(s0 + s1) * (-1.f / D_);
            s0 = 0.f; s1 = 0.f;
#pragma unroll
            for (int i = 0; i < 18; ++i) {
                float a = bflo(xr[i]), b = bfhi(xr[i]);
                asm volatile("v_add_f32 %0, %0, %4\n\tv_add_f32 %1, %1, %4\n\tv_fmac_f32 %2, %0, %0\n\tv_fmac_f32 %3, %1, %1" : "+v"(a), "+v"(b), "+v"(s0), "+v"(s1) : "v"(nm));
            }
            const float rstd = 1.0f / sqrtf(row16_sum(s0 + s1) * (1.f / D_) + 1e-5f);
            tag = __builtin_amdgcn_readfirstlane(epoch) + 1u;
            const u32x4 gr = {__float_as_uint(nm), tag, __float_as_uint(rstd), tag};
            __builtin_amdgcn_raw_buffer_store_b128(gr, rs_ws, r16 == 0 ? WS_STAT + (u32)(bid * 8 + 4 * ax + q) * 16u : 0x80000000u, 0, 16);
            HOLD(gr);
            lds_st<u32x2>(L_TAG, (u32x2){tag, 0u});
            WAIT_L(0);
            SB();
        };
        stats();
        ABAR();
        STAMP(1);
        if (!is_ab) {
            if (ax == 0) {
                wload();
                u32x4 s0, s1;
#pragma unroll
                for (int d = 0; d < 4; ++d) { const u32 v = ((l >> 1) & 3) == d ? ((l & 1) ? 0x3f800000u : 0x3f80u) : 0u; s0[d] = (l & 8) ? 0u : v; s1[d] = (l & 8) ? v : 0u; }
                PIN(s0); PIN(s1);
                f32x4 c1 = {0.f, 0.f, 0.f, 0.f}, c2 = c1;
                auto gm = [&](int i) {
                    c1 = mfma(__builtin_bit_cast(bf16x8, s0), __builtin_bit_cast(bf16x8, wv[3 * i]), c1);
                    c2 = mfma(__builtin_bit_cast(bf16x8, s0), __builtin_bit_cast(bf16x8, wv[3 * i + 2]), c2);
                    c1 = mfma(__builtin_bit_cast(bf16x8, s1), __builtin_bit_cast(bf16x8, wv[3 * i + 1]), c1);
                };
#pragma unroll
                for (int i = 0; i < 9; ++i) {
                    if (i == CS2) { PIN(c1); PIN(c2); ABAR(); }
                    gm(i);
                }
                PIN(c1); PIN(c2);
                ABAR();
                const u32 cpo = WS_CP + (u32)((j * 3 + nb) * 8) * 1536u;
                {
                    const bool hi = l & 8;
                    float k1a = hi ? c1[2] : c1[0], k1b = hi ? c1[3] : c1[1], g1a = hi ? c1[0] : c1[2], g1b = hi ? c1[1] : c1[3];
                    float k2a = hi ? c2[2] : c2[0], k2b = hi ? c2[3] : c2[1], g2a = hi ? c2[0] : c2[2], g2b = hi ? c2[1] : c2[3];
                    k1a += ror8(g1a); k1b += ror8(g1b); k2a += ror8(g2a); k2b += ror8(g2b);
                    const u32 o = cpo + (u32)mb * 1536u;
                    const u32x4 g1 = {__float_as_uint(k1a), tag, __float_as_uint(k1b), tag}, g2 = {__float_as_uint(k2a), tag, __float_as_uint(k2b), tag};
                    __builtin_amdgcn_raw_buffer_store_b128(g1, rs_ws, l16, o, 16);
                    HOLD(g1);
                    __builtin_amdgcn_raw_buffer_store_b128(g2, rs_ws, l < 32 ? l16 + 1024u : 0x80000000u, o, 16);
                    HOLD(g2);
                }
                warmC(0, WA1);
                ABAR();
                warmC(WA1, WA2);
                ABAR();
                sg_load();
                warmC(WA2, WA3);
                ABAR();
                sg_done();
                ABAR();
                u32x4 pa[8], pb[8];
                auto pload = [&]() {
#pragma unroll
                    for (int k = 0; k < 8; ++k) {
                        pa[k] = __builtin_amdgcn_raw_buffer_load_b128(rs_ws, l16, cpo + (u32)k * 1536u, 16);
                        pb[k] = __builtin_amdgcn_raw_buffer_load_b128(rs_ws, (l16 & 511u) + 1024u, cpo + (u32)k * 1536u, 16);
                    }
                };
                pload();
                ABAR();
#pragma unroll 1
                for (int it = 0; it < SPIN_MAX; ++it) {
                    u32 bad = 0;
#pragma unroll
                    for (int k = 0; k < 8; ++k) bad |= (pa[k][1] ^ tag) | (pa[k][3] ^ tag) | (pb[k][1] ^ tag) | (pb[k][3] ^ tag);
                    if (__all(bad == 0)) break;
                    __builtin_amdgcn_s_sleep(1);
                    pload();
                }
                ABAR();
                {
#pragma unroll
                    for (int k = 1; k < 8; ++k)
                        asm volatile("v_add_f32 %0, %0, %4\n\tv_add_f32 %1, %1, %5\n\tv_add_f32 %2, %2, %6\n\tv_add_f32 %3, %3, %7"
                                     : "+v"(pa[0][0]), "+v"(pa[0][2]), "+v"(pb[0][0]), "+v"(pb[0][2]) : "v"(pa[k][0]), "v"(pa[k][2]), "v"(pb[k][0]), "v"(pb[k][2]));
                    const u32x2 t1 = {pa[0][0], pa[0][2]}, t2 = {pb[0][0], pb[0][2]};
                    const u32 c = 8 * (l & 7) + 4 * (q & 1) + 2 * ((l >> 3) & 1);
                    lds_st<u32x2>(L_CS + (64 * (q >> 1) + c) * 4, t1);
                    lds_st<u32x2>(L_CS + (128 + c) * 4, t2);
                    WAIT_L(0);
                }
                ABAR();
            } else {
                const char* pw = (const char*)w2 + (size_t)(576 * j + 24 * (3 * mb + nb)) * 1152u;
                const int mat = j >> 2, u = ((j & 3) * 8 + mb) * 3 + nb;
                u32x4 da[8];
                u32x2 db[2];
                sfor<8>([&](auto t) {
                    if (t == DH) {
                        const rsrc_t rs_d = mk_rsrc(mat ? Di : Dr, u < 72 ? D_ * D_ * 2 : 0);
#pragma unroll
                        for (int i = 0; i < 8; ++i) da[i] = __builtin_amdgcn_raw_buffer_load_b128(rs_d, (u32)q * 1152u + 16u * r16 + 256u * (i & 3), (u32)(8 * u + 4 * (i >> 2)) * 1152u, 0);
#pragma unroll
                        for (int i = 0; i < 2; ++i) db[i] = __builtin_amdgcn_raw_buffer_load_b64(rs_d, (u32)q * 1152u + 8u * r16 + 1024u, (u32)(8 * u + 4 * i) * 1152u, 0);
                    }
                    if (t == DH + DHD) {
                        float r0 = dot1(db[0][1], dot1(db[0][0], 0.f)), r1 = dot1(db[1][1], dot1(db[1][0], 0.f));
#pragma unroll
                        for (int i = 0; i < 16; ++i) { r0 = dot1(da[i >> 2][i & 3], r0); r1 = dot1(da[4 + (i >> 2)][i & 3], r1); }
                        r0 = row16_sum(r0); r1 = row16_sum(r1);
                        const u32x2 gd = {__float_as_uint((r16 & 1) ? r1 : r0), tag};
                        __builtin_amdgcn_raw_buffer_store_b64(gd, rs_ws, (r16 < 2 && u < 72) ? WS_DS + (u32)(576 * mat + 8 * u + 4 * r16 + q) * 8u : 0x80000000u, 0, 16);
                        HOLD(gd);
                    }
                    if (t == 4) { sg_done(); WAIT_L(0); }
                    if (t == 3) sg_load();
#pragma unroll
                    for (int k = 0; k < XN; ++k) {
                        constexpr int nu = (9 - XS) * 4;
                        const int i = XN * (t - XW) + k;
                        if (t >= XW && 3 * i < nu) {
                            const u32 u = min((u32)(3 * i + nb), (u32)(nu - 1));
                            warm1((const char*)x + (u32)(256 * mb + 64 * (u & 3)) * 1152u + 128u * (XS + (u >> 2)), (u32)l * 1152u);
                        }
                    }
                    constexpr int wb[9] = {WB};
                    warmC(wb[t], wb[t + 1]);
#pragma unroll
                    for (int k = 0; k < W2N; ++k) {
                        constexpr int n = W2N * (t - W2S - (t > 3 ? 1 : 0));
                        if (t >= W2S && t != 3 && n + k < 27) warm(pw + (n + k) * 1024, l16);
                    }
                    ABAR();
                });
            }
            {
                constexpr int wb[9] = {WB}, r = (86 - wb[8]) / 4;
                if (ax) warmC(wb[8], wb[8] + r); else warmC(wb[8] + r, wb[8] + 2 * r);
                WAIT_L(0);
                ABAR();
                if (ax) warmC(wb[8] + 2 * r, wb[8] + 3 * r); else warmC(wb[8] + 3 * r, 86);
            }
            ABAR();
            STAMP(2);
            const u32 hd = WS_H + (u32)((j * 8 + mb) * 9 + 3 * nb) * 32768u + ax * 16384u;
            char* fh = ws + WS_FH + ((j * 8 + mb) * 16 + 3 * nb) * FS + ax * HS + 4 * l;
            u32x4 va[16], vb[16];
            auto rd = [&](u32x4 (&v)[16], u32 a) {
#pragma unroll
                for (int i = 0; i < 16; ++i) DSR128(v[i], a, 1024 * i);
                asm volatile("s_waitcnt lgkmcnt(0)" : "+v"(v[0]), "+v"(v[1]), "+v"(v[2]), "+v"(v[3]), "+v"(v[4]), "+v"(v[5]), "+v"(v[6]), "+v"(v[7]),
                             "+v"(v[8]), "+v"(v[9]), "+v"(v[10]), "+v"(v[11]), "+v"(v[12]), "+v"(v[13]), "+v"(v[14]), "+v"(v[15]) :: "memory");
            };
            auto wr = [&](u32x4 (&v)[16], int sl, int i0, int i1) {
#pragma unroll
                for (int i = 0; i < 16; ++i) if (i >= i0 && i < i1) __builtin_amdgcn_raw_buffer_store_b128(v[i], rs_ws, l16 | XA, hd + sl * 32768u + i * 1024u, HAUX);
                asm volatile("s_nop 1" :: "v"(v[i1 - 1]), "v"(v[(i1 - 2) & 15]), "v"(v[(i1 - 3) & 15]), "v"(v[(i1 - 4) & 15]), "v"(v[(i1 - 5) & 15]) : "memory");
            };
            const u32 ha = ax * 16384u + l16;
            {
                constexpr int pc[13] = {0, 0, PC};
                constexpr auto ft = [](int k) { constexpr int c[13] = {0, 0, PC}; int t = 1; while (t < 11 && c[t + 2 - PL] < 16 * (k + 1)) ++t; return t; };
                rd(va, ha);
                sfor<12>([&](auto t) {
                    sfor<3>([&](auto k) {
                        if constexpr (ft(k) == t) {
                            constexpr int fl = (k > 0 && pc[ft(k - 1) + 1] >= 16 * (k + 1) ? 1 : 0) + (k > 1 && pc[ft(k - 2) + 1] >= 16 * (k + 1) ? 1 : 0);
                            asm volatile("s_waitcnt vmcnt(%0) ; warm-up retired" :: "i"(pc[t + 1] - 16 * (k + 1) + fl) : "memory", "v152", "v153", "v154", "v155");
                            if (l < FW) st_flag(fh + k * FS, tag);
                        }
                    });
                    if constexpr (t < 11) {
                        constexpr int a = pc[t + 1], b = pc[t + 2];
                        if constexpr (a < 16) wr(va, 0, a, b < 16 ? b : 16);
                        if constexpr (t == PRB) rd(vb, ha + 32768u);
                        if constexpr (a < 16 && b >= 16) rd(va, ha + 65536u);
                        if constexpr (a < 32 && b > 16) wr(vb, 1, a > 16 ? a - 16 : 0, b < 32 ? b - 16 : 16);
                        if constexpr (b > 32 && a < 48) wr(va, 2, a > 32 ? a - 32 : 0, b - 32);
                        ABAR();
                    }
                });
            }
            STAMP(3);
#pragma unroll 1
            for (int t = 11; t < NB2; ++t) ABAR();
        } else {
            const int mat = j >> 2, u = 9;
            u32x4 dv[18];
            if (u < 9) {
                const rsrc_t rs_d = mk_rsrc(mat ? Di : Dr, D_ * D_ * 2);
#pragma unroll
                for (int i = 0; i < 18; ++i) dv[i] = __builtin_amdgcn_raw_buffer_load_b128(rs_d, (u32)r16 * 1152u + 16u * q + 64u * i, (u32)(144 * cn + 16 * u) * 1152u, 0);
            }
            ABAR(); ABAR();
            if (u < 9) {
                const u32x4 one4 = {0x3f803f80u, 0x3f803f80u, 0x3f803f80u, 0x3f803f80u};
                f32x4 r0 = {0.f, 0.f, 0.f, 0.f}, r1 = r0;
#pragma unroll
                for (int i = 0; i < 9; ++i) {
                    r0 = mfma(__builtin_bit_cast(bf16x8, one4), __builtin_bit_cast(bf16x8, dv[2 * i]), r0);
                    r1 = mfma(__builtin_bit_cast(bf16x8, one4), __builtin_bit_cast(bf16x8, dv[2 * i + 1]), r1);
                }
                if (q == 0) __builtin_amdgcn_raw_buffer_store_b64((u32x2){__float_as_uint(r0[0] + r1[0]), tag}, rs_ws, WS_DS + (u32)(576 * mat + 144 * cn + 16 * u + r16) * 8u, 0, 16);
            }
            ABAR();
            sg_load();
            ABAR(); ABAR();
            sg_done();
            u32x2 dg[2];
            const int rho0 = ax ? 128 + r16 : l, rho1 = ax ? 128 + r16 : 64 + l;
            const u32 do0 = WS_DS + (u32)(576 * mat + 144 * cn + colmap(cn, rho0 >> 4) + r16) * 8u, do1 = WS_DS + (u32)(576 * mat + 144 * cn + colmap(cn, rho1 >> 4) + r16) * 8u;
            auto dl = [&]() { dg[0] = __builtin_amdgcn_raw_buffer_load_b64(rs_ws, do0, 0, 16); dg[1] = __builtin_amdgcn_raw_buffer_load_b64(rs_ws, do1, 0, 16); };
            ABAR();
            dl();
            ABAR(); ABAR();
#pragma unroll 1
            for (int it = 0; it < SPIN_MAX; ++it) {
                if (__all(dg[0][1] == tag && dg[1][1] == tag)) break;
                __builtin_amdgcn_s_sleep(1);
                dl();
            }
            lds_st<u32>(L_CS + rho0 * 4, dg[0][0]); lds_st<u32>(L_CS + rho1 * 4, dg[1][0]);
            WAIT_L(0);
            ABAR();
            ABAR();
            STAMP(2);
            {
                u32x4 v[27];
                const u32 ra = L_B + ax * 9216u + l16, ad = WS_AB + (u32)((j * 4 + cn) * 16 + 8 * (mb >> 2) + ax) * 9216u;
                char* fa = ws + WS_FAB + (j * 8 + mb) * 4 * FS + ax * HS + 4 * l;
#pragma unroll
                for (int i = 0; i < 27; ++i) DSR128(v[i], ra, ((i / 9 + 2) % 3) * 18432 + (i % 9) * 1024);
#pragma unroll
                for (int i = 0; i < 27; ++i) {
                    if (i % 9 == 0) asm volatile("s_waitcnt lgkmcnt(%9)" : "+v"(v[i]), "+v"(v[i + 1]), "+v"(v[i + 2]), "+v"(v[i + 3]), "+v"(v[i + 4]), "+v"(v[i + 5]), "+v"(v[i + 6]), "+v"(v[i + 7]), "+v"(v[i + 8]) : "i"(i ? 0 : 15) : "memory");
                    if (i == 18) {
                        ABAR();
                        WAIT_V(9); if (l < FW) st_flag(fa, tag);
                    }
                    __builtin_amdgcn_raw_buffer_store_b128(v[i], rs_ws, l16, ad + (i / 9) * 18432u + (i % 9) * 1024u, 16);
                }
                WAIT_V(10); if (l < FW) st_flag(fa + FS, tag);
                WAIT_V(1); if (l < FW) st_flag(fa + 2 * FS, tag);
                ABAR();
            }
            const bool bump = bid == 255 && ax == 1;
            u32 tg[32];
            auto tl = [&]() {
#pragma unroll
                for (int k = 0; k < 32; ++k) tg[k] = __builtin_amdgcn_raw_buffer_load_b32(rs_ws, l16 + 4u, WS_STAT + k * 1024u, 16);
            };
            if (bump) tl();
            ABAR(); ABAR(); ABAR();
            if (bump) {
#pragma unroll 1
                for (int it = 0; it < SPIN_MAX; ++it) {
                    u32 bad = 0;
#pragma unroll
                    for (int k = 0; k < 32; ++k) bad |= tg[k] ^ tag;
                    if (__all(bad == 0)) break;
                    __builtin_amdgcn_s_sleep(1);
                    tl();
                }
                if (l == 0) st_flag(ws + WS_EPOCH, tag);
            }
#pragma unroll 1
            for (int t = 5; t < NB2; ++t) ABAR();
        }
        __builtin_amdgcn_s_waitcnt(0x0f70);
        ABAR();
    }

    if (w >= 3) {
        const u32 l16 = lv;
        const int l = lv >> 4, q = l >> 4, r16 = l & 15;
        const int owm = j & 3, own = j >> 2, b = 11 - w;
        const int P = (b < 8) ? (b >> 2) : 0, nf = (b < 8) ? 4 * own + (b & 3) : 8;
        const int trow = 256 * mb + 64 * owm + 32 * ((P + own) & 1) + r16, col = 144 * cn + colmap(cn, nf) + 4 * q;
        const u32x2 x0 = *(const u32x2*)(x + (size_t)trow * D_ + col), x1 = *(const u32x2*)(x + (size_t)(trow + 16) * D_ + col);
        const u32x4 o0 = lds_ld<u32x4>(b * 1024u + l16);
        const u32x4 xx = {x0[0], x0[1], x1[0], x1[1]};
        f32x2 sum[4];
#pragma unroll
        for (int d = 0; d < 4; ++d) { sum[d] = add2(o0[d], (f32x2){bflo(xx[d]), bfhi(xx[d])}); PIN(sum[d]); }
        u32x4 p[7];
#pragma unroll
        for (int k = 0; k < 7; ++k) { p[k] = (u32x4){0, 0, 0, 0}; PIN(p[k]); }
        {
            const u32 fpo = WS_FP + (u32)((g * 8 + j) * 8 + ((j + 1 + l) & 7)) * PS;
            u32 fv = ~tag;
            const char* pb = ws + WS_P + (u32)(((g * 8 + j) * 9 + b) * 8) * 1024u;
            u32 done = 0;
#pragma unroll 1
            for (int it = 0; it < SPIN_MAX; ++it) {
                u32 m;
                if (w == 11) {
                    asm volatile("s_mov_b64 exec, %3\n\tglobal_load_dword %0, %1, %2 sc1\n\ts_mov_b64 exec, -1\n\ts_waitcnt vmcnt(0)"
                                 : "+v"(fv) : "v"(fpo), "s"(ws), "s"((unsigned long long)(done ^ 127u)) : "memory");
                    m = (u32)__builtin_amdgcn_ballot_w64(fv == tag) & 127u;
                    *(volatile LAS u32*)((LAS char*)smem + L_TAG + 4) = m;
                } else m = UNI(*(volatile LAS u32*)((LAS char*)smem + L_TAG + 4));
                m &= ~done;
#pragma unroll
                for (int k = 0; k < 7; ++k)
                    if ((m >> k) & 1u) asm volatile("global_load_dwordx4 %0, %1, %2 sc1" : "+v"(p[k]) : "v"(l16), "s"(pb + ((j + 1 + k) & 7) * 1024) : "memory");
                done |= m;
                if (done == 127u) break;
                if (w == 11) __builtin_amdgcn_s_sleep(FSL); else __builtin_amdgcn_s_sleep(1);
            }
            STAMP(6);
            asm volatile("s_waitcnt vmcnt(0)" : "+v"(p[0]), "+v"(p[1]), "+v"(p[2]), "+v"(p[3]), "+v"(p[4]), "+v"(p[5]), "+v"(p[6]) :: "memory");
        }
#pragma unroll
        for (int k = 0; k < 7; ++k)
#pragma unroll
            for (int d = 0; d < 4; ++d) sum[d] = add2(p[k][d], sum[d]);
        {
            const rsrc_t rs_o = mk_rsrc(out, S_ * D_ * 2);
            const auto s0 = __builtin_amdgcn_permlane16_swap(pk2(sum[0][0], sum[0][1]), pk2(sum[2][0], sum[2][1]), false, false);
            const auto s1 = __builtin_amdgcn_permlane16_swap(pk2(sum[1][0], sum[1][1]), pk2(sum[3][0], sum[3][1]), false, false);
            __builtin_amdgcn_raw_buffer_store_b128((u32x4){s0[0], s1[0], s0[1], s1[1]}, rs_o, (u32)((trow + 16 * (q & 1)) * D_ + col - 4 * (q & 1)) * 2u, 0, OAUX);
        }
    }
}

extern "C" void kernel_launch(void* const* d_in, const int* in_sizes, int n_in, void* d_out, int out_size, void* d_ws, size_t ws_size,
                              hipStream_t stream) {
    (void)hipFuncSetAttribute((const void*)fused_kernel, hipFuncAttributeMaxDynamicSharedMemorySize, LDS_BYTES);
    hipLaunchKernelGGL(fused_kernel, dim3(256), dim3(768), LDS_BYTES, stream, (const u16*)d_in[0], (const u16*)d_in[1], (const u16*)d_in[2],
                       (const u16*)d_in[3], (const u16*)d_in[4], (const u16*)d_in[6], (const u16*)d_in[7], (u16*)d_out, (char*)d_ws);
}
```
